```python
import torch

def _softmax(x, axis=-1):
    return torch.softmax(x, dim=axis)
def _gelu(x):
    return torch.nn.functional.gelu(x, approximate="tanh")
def _silu(x):
    return torch.nn.functional.silu(x)
def _sigmoid(x):
    return torch.sigmoid(x)
def _layernorm(x, axis=-1, eps=1e-5):
    m = x.mean(dim=axis, keepdim=True)
    v = x.var(dim=axis, keepdim=True, unbiased=False)
    return (x - m) / torch.sqrt(v + eps)
def _scatter_add(tgt, idx, src, axis=0):
    return tgt.clone().index_add_(axis, idx, src)
def _segment_sum(src, seg, n):
    out = torch.zeros((n,) + src.shape[1:], dtype=src.dtype)
    return out.index_add_(0, seg, src)
def _pad(x, pw):
    flat = tuple(v for pair in reversed(pw) for v in pair)
    return torch.nn.functional.pad(x, flat)

kernel_name = 'gated_mix__ssm_conv_h3__dense_activation_relu__prompt'

def setup_inputs():

    _w = (lambda a: (lambda t: t.to(torch.bfloat16) if t.is_floating_point() else t)(torch.as_tensor(a)))
    def _A_setup():
        B, S, D = 1, 2048, 512
        ks = 4
        return {
            "q": _w((lambda *s: torch.randn(*s, dtype=torch.bfloat16) * 0.05)(B, S, D)),
            "k": _w((lambda *s: torch.randn(*s, dtype=torch.bfloat16) * 0.8)(B, S, D)),
            "v": _w((lambda *s: torch.randn(*s, dtype=torch.bfloat16) * 0.8)(B, S, D)),
            "shift_k": _w((lambda *s: torch.randn(*s, dtype=torch.bfloat16) * 0.8)(D, ks) * 0.5),
            "ssm_k": _w((lambda *s: torch.randn(*s, dtype=torch.bfloat16) * 0.8)(D, S) * (1.0 / S ** 0.5)),
            "ssm_D": _w((lambda *s: torch.randn(*s, dtype=torch.bfloat16) * 0.8)(D)),
        }
    _A_in = _A_setup()

    def _B_setup():
        B, S, D, Dff = 1, 2048, 512, 1792
        return {
            "x": _w((lambda *s: torch.randn(*s, dtype=torch.bfloat16) * 0.05)(B, S, D)),
            "w1": _w((lambda *s: torch.randn(*s, dtype=torch.bfloat16) * 0.05)(D, Dff)),
            "w2": _w((lambda *s: torch.randn(*s, dtype=torch.bfloat16) * 0.05)(Dff, D)),
        }
    _B_in = _B_setup()

    return {
        "A_q": _A_in["q"],
        "A_k": _A_in["k"],
        "A_v": _A_in["v"],
        "A_shift_k": _A_in["shift_k"],
        "A_ssm_k": _A_in["ssm_k"],
        "A_ssm_D": _A_in["ssm_D"],
        "B_x": _B_in["x"],
        "B_w1": _B_in["w1"],
        "B_w2": _B_in["w2"],
        "gate_w": _w((lambda *s: torch.randn(*s, dtype=torch.bfloat16) * 0.05)(512) * 16.0),
    }

def reference(A_q, A_k, A_v, A_shift_k, A_ssm_k, A_ssm_D, B_x, B_w1, B_w2, gate_w):
    def _A_ref(q, k, v, shift_k, ssm_k, ssm_D):
        B, S, D = q.shape
        ks = shift_k.shape[-1]
        k_pad = _pad(k, ((0, 0), (ks - 1, 0), (0, 0)))
        k_shift = torch.zeros_like(k)
        for t in range(ks):
            k_shift = k_shift + k_pad[:, t:t + S, :] * shift_k[:, ks - 1 - t][None, None, :]
        kv = k_shift * v
        L = 2 * S
        kv_pad = _pad(kv, ((0, 0), (0, S), (0, 0)))
        kf_pad = _pad(ssm_k, ((0, 0), (0, S)))
        kv_f = (lambda x, axes=(-2,-1): torch.fft.rfft2(x.float(), dim=axes))(kv_pad[:, None, :, :], axes=(1, 2))
        kf_f = (lambda x, axes=(-2,-1): torch.fft.rfft2(x.float(), dim=axes))(kf_pad[:, None, :], axes=(1, 2))
        kf_f = (lambda x, *d: x.permute(*d))(kf_f, 1, 2, 0)
        y_f = kv_f * kf_f[None, :, :, :]
        y = (lambda x, s, axes=(-2,-1): torch.fft.irfft2(x, s=s, dim=axes).to(torch.bfloat16))(y_f, (1, L), axes=(1, 2))[:, 0, :S, :]
        y = y + kv * ssm_D[None, None, :]
        return q * y

    def _B_ref(x, w1, w2):
        h = torch.einsum("bsd,df->bsf", x, w1)
        zero = torch.zeros_like(h)
        h = torch.maximum(h, zero)
        return torch.einsum("bsf,fd->bsd", h, w2)

    x = A_q
    g = _sigmoid((x * gate_w).sum(-1, keepdims=True))
    a = _A_ref(x, A_k, A_v, A_shift_k, A_ssm_k, A_ssm_D)
    b = _B_ref(x, B_w1, B_w2)
    return g * a + (1 - g) * b
```

```cpp
#include <hip/hip_runtime.h>
#include <hip/hip_bf16.h>
#include <stdint.h>
typedef unsigned short u16;
static __device__ __forceinline__ float bf2f(u16 x){ return __uint_as_float(((unsigned)x)<<16); }
static __device__ __forceinline__ u16 f2bf(float f){ __hip_bfloat16 b = __float2bfloat16(f); return *reinterpret_cast<u16*>(&b); }
static __device__ __forceinline__ float rbf(float f){ return bf2f(f2bf(f)); }
#define S_ 2048
#define D_ 512
#define F_ 1792
__global__ void n_kv(const u16* k, const u16* v, const u16* sk, u16* kv){
  int i = blockIdx.x*blockDim.x+threadIdx.x; if(i>=S_*D_) return;
  int t=i/D_, d=i%D_;
  float ks=0.f;
  for(int j=0;j<4;j++){ int s=t+j-3; float kk = s>=0? bf2f(k[s*D_+d]):0.f; ks = rbf(ks + rbf(kk*bf2f(sk[d*4+3-j]))); }
  kv[i]=f2bf(ks*bf2f(v[i]));
}
__global__ void n_conv(const u16* kv, const u16* K, const u16* Dv, const u16* q, float* a){
  int i = blockIdx.x*blockDim.x+threadIdx.x; if(i>=S_*D_) return;
  int t=i/D_, d=i%D_;
  float y=0.f;
  for(int s=0;s<=t;s++) y += bf2f(K[d*S_+t-s])*bf2f(kv[s*D_+d]);
  y = rbf(y);
  y = rbf(y + rbf(bf2f(kv[i])*bf2f(Dv[d])));
  a[i]=rbf(bf2f(q[i])*y);
}
__global__ void n_g1(const u16* x, const u16* w1, u16* h){
  int i = blockIdx.x*blockDim.x+threadIdx.x; if(i>=S_*F_) return;
  int t=i/F_, f=i%F_;
  float s=0.f; for(int k=0;k<D_;k++) s+=bf2f(x[t*D_+k])*bf2f(w1[k*F_+f]);
  h[i]=f2bf(fmaxf(s,0.f));
}
__global__ void n_g2(const u16* h, const u16* w2, const u16* x, const u16* gw, const float* a, u16* out){
  int i = blockIdx.x*blockDim.x+threadIdx.x; if(i>=S_*D_) return;
  int t=i/D_, d=i%D_;
  float s=0.f; for(int k=0;k<F_;k++) s+=bf2f(h[t*F_+k])*bf2f(w2[k*D_+d]);
  float b=rbf(s);
  float gs=0.f; for(int k=0;k<D_;k++) gs+=rbf(bf2f(x[t*D_+k])*bf2f(gw[k]));
  gs=rbf(gs);
  float g=rbf(1.f/(1.f+__expf(-gs)));
  float og=rbf(1.f-g);
  out[i]=f2bf(rbf(g*a[i])+rbf(og*b));
}
#define NAIVE_NAME kernel_launch
extern "C" void NAIVE_NAME(void* const* d_in, const int* in_sizes, int n_in, void* d_out, int out_size, void* d_ws, size_t ws_size, hipStream_t stream){
  const u16* q=(const u16*)d_in[0]; const u16* k=(const u16*)d_in[1]; const u16* v=(const u16*)d_in[2];
  const u16* sk=(const u16*)d_in[3]; const u16* K=(const u16*)d_in[4]; const u16* Dv=(const u16*)d_in[5];
  const u16* w1=(const u16*)d_in[7]; const u16* w2=(const u16*)d_in[8]; const u16* gw=(const u16*)d_in[9];
  char* ws=(char*)d_ws;
  u16* kv=(u16*)ws; float* a=(float*)(ws+(4<<20)); u16* h=(u16*)(ws+(16<<20));
  n_kv<<<S_*D_/256,256,0,stream>>>(k,v,sk,kv);
  n_conv<<<S_*D_/256,256,0,stream>>>(kv,K,Dv,q,a);
  n_g1<<<S_*F_/256,256,0,stream>>>(q,w1,h);
  n_g2<<<S_*D_/256,256,0,stream>>>(h,w2,q,gw,a,(u16*)d_out);
}
```

```cpp
#include <hip/hip_runtime.h>
#include <stdint.h>

typedef unsigned short u16;
typedef unsigned long long u64;
using bf16x8 = __attribute__((ext_vector_type(8))) short;
using s16x4  = __attribute__((ext_vector_type(4))) short;
using s16x2  = __attribute__((ext_vector_type(2))) short;
using f32x2  = __attribute__((ext_vector_type(2))) float;
using f32x4  = __attribute__((ext_vector_type(4))) float;
using f32x16 = __attribute__((ext_vector_type(16))) float;
using u32x4  = __attribute__((ext_vector_type(4))) unsigned;
using u32x2  = __attribute__((ext_vector_type(2))) unsigned;
using bf2    = __attribute__((ext_vector_type(2))) __bf16;
#define LAS __attribute__((address_space(3)))
#define GAS __attribute__((address_space(1)))
typedef GAS u64 gu64;
typedef short v4i16_t __attribute__((ext_vector_type(4)));
typedef __amdgpu_buffer_rsrc_t rsrc_t;

constexpr int S_ = 2048, D_ = 512, F_ = 1792;
constexpr unsigned WS_EPOCH = 0;
constexpr unsigned WS_FKV = 4096;
constexpr unsigned WS_FH  = 8192;
constexpr unsigned WS_FY  = 12288;
constexpr unsigned WS_FG  = 16384;
constexpr unsigned WS_G   = 32768;
constexpr unsigned WS_KVT = 1u << 20;
constexpr unsigned WS_Y   = 3u << 20;
constexpr unsigned WS_H   = 5u << 20;
constexpr unsigned WS_END = WS_H + 64u * 112u * 1024u;
constexpr unsigned L_KC   = 98304;
constexpr unsigned L_U    = 131584;
constexpr unsigned L_X0   = 140800;
constexpr unsigned L_CNT  = 157184;
constexpr unsigned L_JUNK = 157248;
constexpr unsigned L_END  = 157504;
constexpr unsigned KCS = 4160;
#define SLOT(s) ((unsigned)((((s) + 3) & 3) * 32768))
enum { C_PROG = 0, C_H = 16, C_TR = 20, C_KV = 24, C_U = 28, C_PA = 32, C_PB = 40, C_Y = 48, C_TAG = 56 };

#define RLX_AGENT __ATOMIC_RELAXED, __HIP_MEMORY_SCOPE_AGENT
#define SBAR() __builtin_amdgcn_sched_barrier(0)
#define CFENCE() asm volatile("" ::: "memory")
#define MFMA32(a, b, c) __builtin_amdgcn_mfma_f32_32x32x16_bf16(a, b, c, 0, 0, 0)
#define MFMA16(a, b, c) __builtin_amdgcn_mfma_f32_16x16x32_bf16(a, b, c, 0, 0, 0)
#define DI __device__ __forceinline__

DI unsigned cvtpk(float lo, float hi) { return __builtin_bit_cast(unsigned, __builtin_convertvector(f32x2{lo, hi}, bf2)); }
DI float lo_f(unsigned x) { return __uint_as_float(x << 16); }
DI float hi_f(unsigned x) { return __uint_as_float(x & 0xffff0000u); }
DI float dot2(unsigned a, unsigned b, float c) { return __builtin_amdgcn_fdot2_f32_bf16(__builtin_bit_cast(bf2, a), __builtin_bit_cast(bf2, b), c, false); }
template <unsigned LO, unsigned SO> DI void dma(unsigned voff, rsrc_t r, unsigned sb, unsigned lb) {
    unsigned t;
    asm volatile("s_add_u32 m0, %4, %5\n\ts_add_u32 %0, %3, %6\n\tbuffer_load_dwordx4 %1, %2, %0 offen lds"
                 : "=&s"(t) : "v"(voff), "s"(r), "s"(sb), "s"(lb), "n"(LO), "n"(SO) : "memory", "scc");
}
DI void touch(const void* p, unsigned lb) {
    asm volatile("s_mov_b32 m0, %1\n\ts_nop 0\n\tglobal_load_lds_dword %0, off" :: "v"(p), "s"(lb) : "memory");
}
DI s16x4 vtr(unsigned a) { return __builtin_bit_cast(s16x4, __builtin_amdgcn_ds_read_tr16_b64_v4i16((LAS v4i16_t*)(uintptr_t)a)); }
DI bf16x8 cat8(s16x4 a, s16x4 b) { return __builtin_shufflevector(a, b, 0, 1, 2, 3, 4, 5, 6, 7); }
DI bf16x8 lds_r128(unsigned a) { return *(const LAS bf16x8*)(uintptr_t)a; }
DI u32x4 lds_r128u(unsigned a) { return *(const LAS u32x4*)(uintptr_t)a; }
DI f32x4 lds_r128f(unsigned a) { return *(const LAS f32x4*)(uintptr_t)a; }
DI unsigned lds_r32(unsigned a) { return *(const LAS unsigned*)(uintptr_t)a; }
DI void lds_w128(unsigned a, u32x4 v) { *(LAS u32x4*)(uintptr_t)a = v; }
DI void lds_w128f(unsigned a, f32x4 v) { *(LAS f32x4*)(uintptr_t)a = v; }
DI void lds_w32(unsigned a, unsigned v) { *(LAS unsigned*)(uintptr_t)a = v; }
DI u64 lds_r64(unsigned a) { return __hip_atomic_load((LAS u64*)(uintptr_t)a, __ATOMIC_RELAXED, __HIP_MEMORY_SCOPE_WAVEFRONT); }
DI u32x4 bld128(rsrc_t r, unsigned vo, int aux) { return aux ? __builtin_amdgcn_raw_buffer_load_b128(r, vo, 0, 16) : __builtin_amdgcn_raw_buffer_load_b128(r, vo, 0, 0); }

#define WAITV(n) asm volatile("s_waitcnt vmcnt(" #n ")" ::: "memory")
#define WAITL() asm volatile("s_waitcnt lgkmcnt(0)" ::: "memory")
#define LBAR() asm volatile("s_waitcnt lgkmcnt(0)\n\ts_barrier" ::: "memory")

#define SPIN_CAP_LOG 18
constexpr unsigned SPIN_CAP = 1u << SPIN_CAP_LOG;

DI void cnt_add(unsigned a) {
    asm volatile("s_mov_b64 exec, 1\n\tds_add_u32 %0, %1\n\ts_mov_b64 exec, -1" :: "v"(a), "v"(1u) : "memory");
}
DI unsigned cnt_add_rtn(unsigned a) {
    unsigned r;
    asm volatile("s_mov_b64 exec, 1\n\tds_add_rtn_u32 %0, %1, %2\n\ts_mov_b64 exec, -1\n\ts_waitcnt lgkmcnt(0)" : "=&v"(r) : "v"(a), "v"(1u) : "memory");
    return __builtin_amdgcn_readfirstlane(r);
}
DI void cnt_set(unsigned a, unsigned v) {
    asm volatile("s_mov_b64 exec, 1\n\tds_write_b32 %0, %1\n\ts_mov_b64 exec, -1" :: "v"(a), "v"(v) : "memory");
}
DI void cnt_wait(unsigned a, unsigned n) {
#pragma unroll 1
    for (unsigned spins = 0; spins < SPIN_CAP * 8; ++spins) {
        CFENCE();
        if (__builtin_amdgcn_readfirstlane(lds_r32(a)) >= n) break;
    }
    CFENCE();
}
DI unsigned min4(u32x4 v) { return __builtin_amdgcn_readfirstlane(min(min(v[0], v[1]), min(v[2], v[3]))); }
DI void prog_wait(unsigned a, unsigned n) {
#pragma unroll 1
    for (unsigned spins = 0; spins < SPIN_CAP * 8; ++spins) {
        CFENCE();
        if (min4(lds_r128u(a)) >= n) break;
    }
    CFENCE();
}
DI void prog_wait_sl(unsigned a, unsigned n) {
#pragma unroll 1
    for (unsigned spins = 0; spins < SPIN_CAP * 8; ++spins) {
        CFENCE();
        if (min4(lds_r128u(a)) >= n) break;
        __builtin_amdgcn_s_sleep(2);
    }
    CFENCE();
}
DI void flag_wait(const gu64* p, u64 f, u64 tag) {
    if (!__all(f == tag)) {
#pragma unroll 1
        for (unsigned spins = 0; spins < SPIN_CAP; ++spins) {
            if (__all(__hip_atomic_load(p, RLX_AGENT) == tag)) break;
            __builtin_amdgcn_s_sleep(1);
        }
    }
    CFENCE();
}

#define STAMP(i)
#define STAMP_OUT()
struct Params {
    const u16* x; const u16* k; const u16* v; const u16* sk; const u16* sK; const u16* sD;
    const u16* w1; const u16* w2; const u16* gw; u16* out; char* ws;
};

__global__ void __launch_bounds__(512, 1) fused_kernel(Params p) {
    extern __shared__ __attribute__((aligned(16))) char smem[];
    const unsigned lds0 = (unsigned)(uintptr_t)(LAS char*)smem;
    const int tid = threadIdx.x, lane = tid & 63;
    const int w8 = __builtin_amdgcn_readfirstlane(tid >> 6), w = w8 & 3;
    const int b = blockIdx.x, xcd = b & 7, j = b >> 3;
    const int R = xcd * 4 + (j >> 3), C = j & 7;
    const bool gemm1 = j < 28;
    const int mt = (xcd >> 1) * 4 + j / 7, nt = (xcd & 1) * 7 + j % 7;
    const int cp = b;
    char* ws = p.ws;
    const rsrc_t wsr = __builtin_amdgcn_make_buffer_rsrc(ws, 0, WS_END, 0x00020000);
    const unsigned cnt = lds0 + L_CNT;
    const int h32 = lane >> 5, blk = (lane >> 4) & 1, q4 = (lane & 15) >> 2, pp = lane & 3;
    if (tid < 16) lds_w32(cnt + 4 * tid, 0u);
    LBAR();
    STAMP(0);

    if (w8 < 4) {
        const rsrc_t w2r = __builtin_amdgcn_make_buffer_rsrc((void*)p.w2, 0, F_ * D_ * 2, 0x00020000);
        const unsigned w2off = (unsigned)(lane >> 3) * 1024u + (unsigned)(((lane & 7) ^ (((lane >> 4) & 1) << 2)) * 16);
        const unsigned w2s = (unsigned)(448 * w) * 1024u + C * 128u;
        const unsigned lx0 = lds0 + L_X0 + w * 4096u, lsw = lds0 + w * 8192u;
#define W2P(ps, i) do { if ((i) < 4) (dma<(i) * 1024u, (28u * (ps) + (i)) * 8192u>(w2off, w2r, w2s, lx0));                      \
        else (dma<(((i) - 4) >> 3) * 32768u + (((i) - 4) & 7) * 1024u, (28u * (ps) + (i)) * 8192u>(w2off, w2r, w2s, lsw)); } while (0)
        if (gemm1) {
            const rsrc_t xr = __builtin_amdgcn_make_buffer_rsrc((void*)p.x, 0, S_ * D_ * 2, 0x00020000);
            const rsrc_t w1r = __builtin_amdgcn_make_buffer_rsrc((void*)p.w1, 0, F_ * D_ * 2, 0x00020000);
            const unsigned xoffE = (unsigned)(lane >> 3) * 1024u + (unsigned)(((lane & 7) ^ ((lane >> 4) & 7)) * 16);
            const unsigned xoffO = xoffE ^ 64u;
            const unsigned woff = (unsigned)(lane >> 4) * 3584u + (unsigned)((((lane & 15) ^ ((lane >> 4) << 2))) * 16);
            const unsigned woffs[4] = {woff, woff ^ 16u, woff ^ 32u, woff ^ 48u};
            const unsigned xs = (unsigned)(128 * mt + 32 * w) * 1024u, w1s = (unsigned)(16 * w) * 3584u + nt * 256u;
            const unsigned lw = lds0 + w * 4096u;
            const int rot = j % 7 + 2 * (j / 7);
            unsigned xss[8], wss[8];
#pragma unroll
            for (int s = 0; s < 8; ++s) { const unsigned sr_ = (unsigned)(s + rot) & 7u; xss[s] = xs + sr_ * 128u; wss[s] = w1s + sr_ * (64u * 3584u); }
#define G1P(s, i) do { if (((i) & 1) == 0) (dma<SLOT(s) + ((i) >> 1) * 1024u, ((i) >> 1) * 8192u>(((i) & 2) ? xoffO : xoffE, xr, xss[s], lw)); \
            else (dma<SLOT(s) + 16384u + ((i) >> 1) * 1024u, ((i) >> 1) * 4u * 3584u>(woffs[(i) >> 1], w1r, wss[s], lw)); } while (0)
#define G1S(s) do { G1P(s, 0); G1P(s, 1); G1P(s, 2); G1P(s, 3); G1P(s, 4); G1P(s, 5); G1P(s, 6); G1P(s, 7); } while (0)
            G1S(0); G1S(1); G1S(2);
            const int wn = w >> 1, wm = w & 1;
            unsigned aaddr[2][2], baddr[4];
#pragma unroll
            for (int a = 0; a < 2; ++a)
#pragma unroll
                for (int t = 0; t < 2; ++t) {
                    const int row = 8 * h32 + 4 * t + q4, ch = 4 * (2 * wn + a) + 2 * blk + (pp >> 1);
                    aaddr[a][t] = lds0 + 16384 + 256 * row + 16 * (ch ^ ((q4 << 2) | (2 * h32 + t))) + 8 * (pp & 1);
                }
#pragma unroll
            for (int ks = 0; ks < 4; ++ks) {
                const int row = 64 * wm + (lane & 31);
                baddr[ks] = lds0 + row * 128 + 16 * ((2 * ks + h32) ^ ((row >> 1) & 7));
            }
            f32x16 acc[2][2];
#pragma unroll
            for (int a = 0; a < 2; ++a)
#pragma unroll
                for (int c = 0; c < 2; ++c) acc[a][c] = f32x16{};
            bf16x8 af[3][2], bfr[3][2];
            unsigned aah[2][2], bah[4];
#pragma unroll
            for (int i = 0; i < 4; ++i) { aah[i >> 1][i & 1] = aaddr[i >> 1][i & 1] + 65536u; bah[i] = baddr[i] + 65536u; asm volatile("" : "+v"(aah[i >> 1][i & 1]), "+v"(bah[i])); }
#define G1_AA(a, t, so, ks) ((so) >= 65536u ? aah[a][t] + ((so) - 65536u) + (ks) * 4096 : aaddr[a][t] + (so) + (ks) * 4096)
#define G1_BA(c, so, ks) ((so) >= 65536u ? bah[ks] + ((so) - 65536u) + (c) * 4096 : baddr[ks] + (so) + (c) * 4096)
#define G1_RD(buf, i, so, ks) do { af[buf][i] = cat8(vtr(G1_AA(i, 0, so, ks)), vtr(G1_AA(i, 1, so, ks))); bfr[buf][i] = lds_r128(G1_BA(i, so, ks)); SBAR(); } while (0)
#define G1_M(buf, a, c) do { acc[a][c] = MFMA32(af[buf][a], bfr[buf][c], acc[a][c]); SBAR(); } while (0)
#define G1_GRP(buf, F0, F1, F2, F3) do { G1_M(buf, 0, 0); F0; SBAR(); G1_M(buf, 0, 1); F1; SBAR(); G1_M(buf, 1, 0); F2; SBAR(); G1_M(buf, 1, 1); F3; SBAR(); } while (0)
            const unsigned myprog = cnt + C_PROG + 4 * w;
            WAITV(16);
            cnt_set(myprog, 1u);
            prog_wait(cnt + C_PROG, 1u);
            STAMP(1);
            G1_RD(0, 0, SLOT(0), 0); G1_RD(0, 1, SLOT(0), 0); G1_RD(1, 0, SLOT(0), 1); G1_RD(1, 1, SLOT(0), 1);
            u32x4 cv;
#define NOP ((void)0)
#define BUF(s, ks) ((4 * (s) + (ks)) % 3)
#define G1_STAGE(s, D0, D1, D2, D3, D4, D5, D6, D7, WV) do {                                                                 \
            G1_GRP(BUF(s, 0), G1_RD(BUF(s, 2), 0, SLOT(s), 2), D0, G1_RD(BUF(s, 2), 1, SLOT(s), 2), D1);                      \
            G1_GRP(BUF(s, 1), G1_RD(BUF(s, 3), 0, SLOT(s), 3), D2, G1_RD(BUF(s, 3), 1, SLOT(s), 3), D3);                      \
            WV; cnt_set(myprog, (s) + 2u); SBAR();                                                                            \
            G1_GRP(BUF(s, 2), D4, cv = lds_r128u(cnt + C_PROG), D5, NOP);                                                     \
            if (min4(cv) < (s) + 2u) prog_wait(cnt + C_PROG, (s) + 2u);                                                       \
            if ((s) < 7) { G1_RD(BUF(s, 4), 0, SLOT((s) + 1), 0);                                                             \
                G1_GRP(BUF(s, 3), G1_RD(BUF(s, 4), 1, SLOT((s) + 1), 0); D6, G1_RD(BUF(s, 5), 0, SLOT((s) + 1), 1), G1_RD(BUF(s, 5), 1, SLOT((s) + 1), 1), D7); } \
            else G1_GRP(BUF(s, 3), D6, NOP, D7, NOP); } while (0)
#define G1_STD(s) G1_STAGE(s, G1P(s + 3, 0), G1P(s + 3, 1), G1P(s + 3, 2), G1P(s + 3, 3), G1P(s + 3, 4), G1P(s + 3, 5), G1P(s + 3, 6), G1P(s + 3, 7), WAITV(12))
            G1_STD(0); G1_STD(1);
            G1_STD(2); G1_STD(3);
            G1_STD(4);
            G1_STAGE(5, W2P(0, 0), W2P(0, 1), W2P(0, 2), W2P(0, 3), NOP, NOP, NOP, NOP, WAITV(12));
            G1_STAGE(6, W2P(0, 4), W2P(0, 5), W2P(0, 6), W2P(0, 7), W2P(0, 8), W2P(0, 9), W2P(0, 10), W2P(0, 11), WAITV(8));
            G1_STAGE(7, W2P(0, 12), W2P(0, 13), W2P(0, 14), W2P(0, 15), W2P(0, 16), W2P(0, 17), W2P(0, 18), W2P(0, 19), NOP);
            STAMP(2);
#pragma unroll
            for (int a = 0; a < 2; ++a)
#pragma unroll
                for (int c = 0; c < 2; ++c)
#pragma unroll
                    for (int s = 0; s < 2; ++s) {
                        u32x4 o;
#pragma unroll
                        for (int i = 0; i < 4; ++i)
                            o[i] = __builtin_bit_cast(unsigned, __builtin_elementwise_max(__builtin_bit_cast(s16x2, cvtpk(acc[a][c][8 * s + 2 * i], acc[a][c][8 * s + 2 * i + 1])), s16x2{0, 0}));
                        const unsigned mb = 4 * mt + 2 * wm + c, ksg = 8 * nt + 4 * wn + 2 * a + s;
                        __builtin_amdgcn_raw_buffer_store_b128(o, wsr, lane * 16u, WS_H + (mb * 112u + ksg) * 1024u, 16);
                        SBAR();
                    }
            STAMP(3);
            W2P(0, 20); W2P(0, 21); W2P(0, 22); W2P(0, 23); W2P(0, 24); W2P(0, 25); W2P(0, 26); W2P(0, 27);
        } else {
            cnt_set(cnt + C_PROG + 4 * w, 10u);
            const int sp = xcd * 4 + (j - 28), sw = (j - 28) * 4 + w;
            const u32x4 gwv = *(const u32x4*)(p.gw + lane * 8);
            u32x4 xv[16];
#pragma unroll
            for (int i = 0; i < 16; ++i) xv[i] = *(const u32x4*)(p.x + (64 * sp + 16 * w + i) * D_ + lane * 8);
            CFENCE();
            float r[16];
#pragma unroll
            for (int i = 0; i < 16; ++i) r[i] = dot2(xv[i][3], gwv[3], dot2(xv[i][2], gwv[2], dot2(xv[i][1], gwv[1], dot2(xv[i][0], gwv[0], 0.f))));
#pragma unroll
            for (int i = 0; i < 8; ++i) { auto s = __builtin_amdgcn_permlane32_swap(__float_as_uint(r[i]), __float_as_uint(r[i + 8]), false, false); r[i] = __uint_as_float(s[0]) + __uint_as_float(s[1]); }
#pragma unroll
            for (int i = 0; i < 4; ++i) { auto s = __builtin_amdgcn_permlane16_swap(__float_as_uint(r[i]), __float_as_uint(r[i + 4]), false, false); r[i] = __uint_as_float(s[0]) + __uint_as_float(s[1]); }
#pragma unroll
            for (int o = 8; o >= 1; o >>= 1)
#pragma unroll
                for (int i = 0; i < 4; ++i) r[i] += __shfl_xor(r[i], o);
            const float gs = pp == 0 ? r[0] : pp == 1 ? r[1] : pp == 2 ? r[2] : r[3];
            const float g = 1.f / (1.f + __expf(-gs));
            __builtin_amdgcn_raw_buffer_store_b32(__float_as_uint(g), wsr, q4 == 0 ? WS_G + (unsigned)(64 * sp + 16 * w + 4 * (lane >> 4) + pp) * 4u : 0xfffffff0u, 0, 16);
            STAMP(2);
            W2P(0, 0); W2P(0, 1); W2P(0, 2); W2P(0, 3); W2P(0, 4); W2P(0, 5); W2P(0, 6); W2P(0, 7); W2P(0, 8); W2P(0, 9);
            W2P(0, 10); W2P(0, 11); W2P(0, 12); W2P(0, 13); W2P(0, 14); W2P(0, 15); W2P(0, 16); W2P(0, 17); W2P(0, 18); W2P(0, 19);
            W2P(0, 20); W2P(0, 21); W2P(0, 22); W2P(0, 23); W2P(0, 24); W2P(0, 25); W2P(0, 26); W2P(0, 27);
            WAITV(0);
            STAMP(3);
        }
        STAMP(4);
        const unsigned w2sw = 128 * (4 * h32 + q4) + 16 * ((2 * blk + (pp >> 1)) ^ ((q4 >> 1) << 2)) + 8 * (pp & 1);
        unsigned wa[2][3];
#pragma unroll
        for (int a = 0; a < 2; ++a) {
            const unsigned o = w2sw ^ (unsigned)((a ^ (w >> 1)) * 64);
            wa[a][0] = lx0 + o; wa[a][1] = lsw + o; wa[a][2] = lsw + 65536u + o;
        }
#define W2ADDR(a, kk) ((kk) < 2 ? wa[a][0] + (kk) * 2048u : (kk) < 10 ? wa[a][1] + (((kk) - 2) >> 2) * 32768u + (((kk) - 2) & 3) * 2048u : wa[a][2] + ((kk) - 10) * 2048u)
#define W2FRAG(a, kk) cat8(vtr(W2ADDR(a, kk)), vtr(W2ADDR(a, kk) + 1024u))
        bf16x8 w2f[14][2];
#define W2GET(k0, k1) do { _Pragma("unroll") for (int kk = k0; kk < k1; ++kk) _Pragma("unroll") for (int a = 0; a < 2; ++a) w2f[kk][a] = W2FRAG(a, kk); WAITL(); } while (0)
        cnt_wait(cnt + C_TR, 4u);
        const u64 tag = lds_r64(cnt + C_TAG);
        WAITV(24);
        W2GET(0, 6);
        W2P(1, 0); W2P(1, 1); W2P(1, 2); W2P(1, 3);
        WAITV(12);
        if (cnt_add_rtn(cnt + C_H) == 3u && lane == 0)
            __hip_atomic_store((gu64*)(ws + (gemm1 ? WS_FH + (unsigned)(mt * 16 + nt) * 8u : WS_FG + (unsigned)(xcd * 4 + j - 28) * 8u)), tag, RLX_AGENT);
        STAMP(5);
        W2P(1, 4); W2P(1, 5); W2P(1, 6); W2P(1, 7); W2P(1, 8); W2P(1, 9); W2P(1, 10); W2P(1, 11);
        W2GET(6, 10);
        W2P(1, 12); W2P(1, 13); W2P(1, 14); W2P(1, 15); W2P(1, 16); W2P(1, 17); W2P(1, 18); W2P(1, 19);
        WAITV(20);
        W2GET(10, 14);
        STAMP(6);
        if (b == 255 && w == 0) {
            const gu64* fp = (const gu64*)(ws + WS_FKV + (unsigned)lane * 8u);
#pragma unroll 1
            for (unsigned spins = 0; spins < SPIN_CAP; ++spins) {
                const u64 f0 = __hip_atomic_load(fp, RLX_AGENT), f1 = __hip_atomic_load(fp + 64, RLX_AGENT), f2 = __hip_atomic_load(fp + 128, RLX_AGENT), f3 = __hip_atomic_load(fp + 192, RLX_AGENT);
                if (__all(f0 == tag && f1 == tag && f2 == tag && f3 == tag)) break;
                __builtin_amdgcn_s_sleep(1);
            }
            if (lane == 0) __hip_atomic_store((gu64*)(ws + WS_EPOCH), tag, RLX_AGENT);
        }
        const gu64* fh = (const gu64*)(ws + WS_FH + (unsigned)((R >> 1) * 16 + ((448 * w) >> 7) + pp) * 8u);
        u64 f;
        asm volatile("global_load_dwordx2 %0, %1, off sc1" : "=v"(f) : "v"(fh) : "memory");
        W2P(1, 20); W2P(1, 21); W2P(1, 22); W2P(1, 23); W2P(1, 24); W2P(1, 25); W2P(1, 26); W2P(1, 27);
        asm volatile("s_waitcnt vmcnt(8)" : "+v"(f) :: "memory");
        flag_wait(fh, f, tag);
        STAMP(7);
        f32x16 acc[2][2];
#pragma unroll
        for (int a = 0; a < 2; ++a)
#pragma unroll
            for (int c = 0; c < 2; ++c) acc[a][c] = f32x16{};
        {
            constexpr int LEAD = 6;
            const unsigned hs0 = WS_H + ((2 * R + (w & 1)) * 112u + 28 * w) * 1024u, hs1 = WS_H + ((2 * R + ((w & 1) ^ 1)) * 112u + 28 * w) * 1024u;
            bf16x8 hf[28][2], wf[2][2];
#define HLD(ks, c) hf[ks][c] = __builtin_bit_cast(bf16x8, __builtin_amdgcn_raw_buffer_load_b128(wsr, lane * 16u, ((c) ? hs1 : hs0) + (ks) * 1024u, 16))
#pragma unroll
            for (int ks = 0; ks < LEAD; ++ks) { HLD(ks, 0); HLD(ks, 1); }
            SBAR();
#pragma unroll
            for (int ks = 0; ks < 28; ++ks) {
                if (ks >= 13 && ks < 27) { wf[(ks + 1) & 1][0] = W2FRAG(0, ks + 1 - 14); wf[(ks + 1) & 1][1] = W2FRAG(1, ks + 1 - 14); SBAR(); }
                const bf16x8 a0 = ks < 14 ? w2f[ks < 14 ? ks : 0][0] : wf[ks & 1][0], a1 = ks < 14 ? w2f[ks < 14 ? ks : 0][1] : wf[ks & 1][1];
                acc[0][0] = MFMA32(a0, hf[ks][0], acc[0][0]); SBAR();
                if (ks + LEAD < 28) { HLD(ks + LEAD < 28 ? ks + LEAD : 0, 0); SBAR(); }
                acc[1][0] = MFMA32(a1, hf[ks][0], acc[1][0]); SBAR();
                acc[0][1] = MFMA32(a0, hf[ks][1], acc[0][1]); SBAR();
                if (ks + LEAD < 28) { HLD(ks + LEAD < 28 ? ks + LEAD : 0, 1); SBAR(); }
                acc[1][1] = MFMA32(a1, hf[ks][1], acc[1][1]); SBAR();
            }
        }
        STAMP(8);
#pragma unroll
        for (int o = 1; o < 4; ++o)
#pragma unroll
            for (int g = 0; g < 4; ++g) {
                const f32x16 v = acc[o >> 1][o & 1];
                lds_w128f(lsw + (o == 3 ? 32768u : (o - 1) * 4096u) + (unsigned)(g * 64 + lane) * 16u, f32x4{v[4 * g], v[4 * g + 1], v[4 * g + 2], v[4 * g + 3]});
            }
        STAMP(9);
        LBAR();
        STAMP(10);
        {
            const unsigned kc = lds0 + L_KC + (w >> 1) * (4 * KCS);
            const float og = __uint_as_float(lds_r32(kc + 12288u + (w & 1) * 256u + lane * 4u));
            u32x2 o2[4];
#pragma unroll
            for (int g = 0; g < 4; ++g) {
                f32x4 s = f32x4{acc[0][0][4 * g], acc[0][0][4 * g + 1], acc[0][0][4 * g + 2], acc[0][0][4 * g + 3]};
#pragma unroll
                for (int o = 1; o < 4; ++o) s += lds_r128f(lds0 + (unsigned)((w ^ o) * 8192) + (o == 3 ? 32768u : (o - 1) * 4096u) + (unsigned)(g * 64 + lane) * 16u);
                const f32x4 ga = lds_r128f(kc + (w & 1) * 4096u + (unsigned)(g * 64 + lane) * 16u);
                o2[g][0] = cvtpk(fmaf(og, s[0], ga[0]), fmaf(og, s[1], ga[1]));
                o2[g][1] = cvtpk(fmaf(og, s[2], ga[2]), fmaf(og, s[3], ga[3]));
            }
            u16* op = p.out + (64 * R + 32 * (w & 1) + (lane & 31)) * D_ + 64 * C + 32 * (w >> 1) + 8 * h32;
#pragma unroll
            for (int g = 0; g < 4; g += 2) {
                auto s0 = __builtin_amdgcn_permlane32_swap(o2[g][0], o2[g + 1][0], false, false);
                auto s1 = __builtin_amdgcn_permlane32_swap(o2[g][1], o2[g + 1][1], false, false);
                *(u32x4*)(op + 8 * g) = u32x4{s0[0], s1[0], s0[1], s1[1]};
            }
        }
        STAMP_OUT();
    } else {
        const int tb = tid - 256;
        __builtin_amdgcn_s_setprio(3);
        const u64 ep = __hip_atomic_load((const gu64*)(ws + WS_EPOCH), RLX_AGENT);
        const rsrc_t kr_ = __builtin_amdgcn_make_buffer_rsrc((void*)p.k, 0, S_ * D_ * 2, 0x00020000);
        u32x4 kr[5], vr[2], sr[4], kA[2]; u32x2 kB[2];
        const int tp = tb >> 3, cc = tb & 7, t0 = 64 * R + 2 * tp, ch = 64 * C + 8 * cc;
#pragma unroll
        for (int i = 0; i < 5; ++i) { const int t = t0 - 3 + i; kr[i] = bld128(kr_, t < 0 ? 0x80000000u : (unsigned)(t * D_ + ch) * 2u, 0); }
#pragma unroll
        for (int i = 0; i < 2; ++i) vr[i] = *(const u32x4*)(p.v + (t0 + i) * D_ + ch);
#pragma unroll
        for (int i = 0; i < 4; ++i) sr[i] = *(const u32x4*)(p.sk + ch * 4 + i * 8);
        CFENCE();
#pragma unroll
        for (int h = 0; h < 2; ++h) {
            const u16* kp = p.sK + (2 * cp + h) * S_;
            kA[h] = *(const u32x4*)(kp + 2040 - 8 * tb);
            kB[h] = *(const u32x2*)(kp + (tb == 255 ? 0 : 2036 - 8 * tb));
        }
        CFENCE();
        const u64 tag = ep + 0x9E3779B97F4A7C15ull;
        if (tb == 0) *(LAS u64*)(uintptr_t)(cnt + C_TAG) = tag;
        STAMP(1);
#pragma unroll
        for (int e = 0; e < 8; ++e) {
            const unsigned s01 = sr[e >> 1][(e & 1) * 2], s23 = sr[e >> 1][(e & 1) * 2 + 1];
            const unsigned sel = (e & 1) ? 0x03020706u : 0x01000504u;
            unsigned pr[4];
#pragma unroll
            for (int i = 0; i < 4; ++i) pr[i] = __builtin_amdgcn_perm(kr[i + 1][e >> 1], kr[i][e >> 1], sel);
            const float r0 = dot2(pr[2], s01, dot2(pr[0], s23, 0.f)), r1 = dot2(pr[3], s01, dot2(pr[1], s23, 0.f));
            const float va = (e & 1) ? hi_f(vr[0][e >> 1]) : lo_f(vr[0][e >> 1]);
            const float vb = (e & 1) ? hi_f(vr[1][e >> 1]) : lo_f(vr[1][e >> 1]);
            lds_w32(lds0 + L_U + (unsigned)((8 * cc + e) * 32 + (tp ^ (cc << 2))) * 4, cvtpk(r0 * va, r1 * vb));
        }
        cnt_add(cnt + C_TR);
        cnt_wait(cnt + C_TR, 4u);
        STAMP(2);
#pragma unroll
        for (int i = 0; i < 2; ++i) {
            const int row = (tb >> 3) + 32 * i, q = tb & 7;
            const u32x4 vv = lds_r128u(lds0 + L_U + (unsigned)(row * 128 + ((q ^ (row >> 3)) * 16)));
            __builtin_amdgcn_raw_buffer_store_b128(vv, wsr, WS_KVT + (unsigned)((64 * C + row) * S_ + 64 * R + 8 * q) * 2, 0, 16);
        }
        CFENCE();
        unsigned Rr[2][6], Mm[2][6];
#pragma unroll
        for (int h = 0; h < 2; ++h) {
            const unsigned W[6] = {tb == 255 ? 0u : kB[h][0], tb == 255 ? 0u : kB[h][1], kA[h][0], kA[h][1], kA[h][2], kA[h][3]};
#pragma unroll
            for (int i = 1; i < 6; ++i) { Rr[h][i] = __builtin_amdgcn_alignbit(W[i], W[i], 16); Mm[h][i] = __builtin_amdgcn_perm(W[i - 1], W[i], 0x07060100u); }
        }
        WAITV(0);
        if (cnt_add_rtn(cnt + C_KV) == 3u && lane == 0) __hip_atomic_store((gu64*)(ws + WS_FKV + (unsigned)(C * 32 + R) * 8u), tag, RLX_AGENT);
        STAMP(3);
        const int tf = 64 * R + 32 * (w & 1) + (lane & 31), chf = 64 * C + 32 * (w >> 1) + 4 * h32;
        u32x2 qv[4];
#pragma unroll
        for (int g = 0; g < 4; ++g) qv[g] = *(const u32x2*)(p.x + tf * D_ + chf + 8 * g);
        const int cw = w >> 1, rh = w & 1, a16 = lane & 15, kq = lane >> 4;
        const float dch = lo_f(p.sD[2 * cp + cw]);
        prog_wait_sl(cnt + C_PROG, 6u);
        STAMP(4);
        const gu64* fk = (const gu64*)(ws + WS_FKV + (unsigned)((cp >> 5) * 32 + 8 * w + (lane & 7)) * 8u);
        const u64 f0 = __hip_atomic_load(fk, RLX_AGENT);
        CFENCE();
#pragma unroll
        for (int h = 0; h < 2; ++h) {
            const unsigned kb = lds0 + L_KC + h * 4 * KCS + tb * 16;
            lds_w128(kb + 0 * KCS, u32x4{Rr[h][5], Rr[h][4], Rr[h][3], Rr[h][2]});
            lds_w128(kb + 1 * KCS, u32x4{Mm[h][5], Mm[h][4], Mm[h][3], Mm[h][2]});
            lds_w128(kb + 2 * KCS, u32x4{Rr[h][4], Rr[h][3], Rr[h][2], Rr[h][1]});
            lds_w128(kb + 3 * KCS, u32x4{Mm[h][4], Mm[h][3], Mm[h][2], Mm[h][1]});
        }
        if (tb < 16) lds_w128(lds0 + L_KC + (tb >> 1) * KCS + 4096 + (tb & 1) * 16, u32x4{0u, 0u, 0u, 0u});
        CFENCE();
        flag_wait(fk, f0, tag);
        STAMP(5);
        u32x4 ur[2];
#pragma unroll
        for (int h = 0; h < 2; ++h) ur[h] = bld128(wsr, WS_KVT + (unsigned)((2 * cp + h) * S_ + 512 * w + 8 * lane) * 2, 1);
        cnt_wait(cnt + C_KV, 4u);
        if (tb < 64) lds_w128(lds0 + L_U + (tb >> 5) * 4608 + (tb & 31) * 16, u32x4{0u, 0u, 0u, 0u});
#pragma unroll
        for (int h = 0; h < 2; ++h) lds_w128(lds0 + L_U + h * 4608 + 512 + (512 * w + 8 * lane) * 2, ur[h]);
        cnt_add(cnt + C_U);
        cnt_wait(cnt + C_U, 4u);
        STAMP(6);
        f32x4 cacc[8];
        {
            const unsigned kaddr = lds0 + L_KC + cw * 4 * KCS + (3 - (a16 & 3)) * KCS + 2 * (2052 - 4 * (a16 >> 2) - 8 * kq) - 256 * rh - 64 * 63;
            const unsigned uaddr = lds0 + L_U + cw * 4608 + 2 * (256 + 16 * a16 + 8 - 8 * kq) - 448 - 256 * rh;
#pragma unroll
            for (int qq = 0; qq < 8; ++qq) cacc[qq] = f32x4{0.f, 0.f, 0.f, 0.f};
            bf16x8 A[2][8], B[2][8];
#define CV_RD(buf, rr, e) do { const unsigned ka = kaddr + 64 * (63 - 8 * (e)) - 64 * (rr); const u64 x0 = lds_r64(ka), x1 = lds_r64(ka + 8);       \
            A[buf][e] = __builtin_bit_cast(bf16x8, u32x4{(unsigned)x0, (unsigned)(x0 >> 32), (unsigned)x1, (unsigned)(x1 >> 32)});                    \
            B[buf][e] = lds_r128(uaddr + 512 * (e) + 64 * (7 - (rr))); } while (0)
#pragma unroll
            for (int e = 0; e < 8; ++e) CV_RD(0, 0, e);
            SBAR();
#pragma unroll
            for (int rr = 0; rr < 4; ++rr) {
#pragma unroll
                for (int m = 0; m < 8; ++m) {
                    if (rr < 3) CV_RD((rr + 1) & 1, rr + 1, m);
#pragma unroll
                    for (int e = 0; e <= m; ++e) if (e + m < 8) cacc[e + m] = MFMA16(A[rr & 1][e], B[rr & 1][m], cacc[e + m]);
#pragma unroll
                    for (int e = 0; e < m; ++e) if (e + m < 8) cacc[e + m] = MFMA16(A[rr & 1][m], B[rr & 1][e], cacc[e + m]);
                    SBAR();
                }
            }
        }
        STAMP(7);
        f32x4 give[4], keep[4];
#pragma unroll
        for (int i = 0; i < 4; ++i)
#pragma unroll
            for (int e = 0; e < 4; ++e) { const float lo = cacc[i][e], hi = cacc[4 + i][e]; give[i][e] = rh ? lo : hi; keep[i][e] = rh ? hi : lo; }
        const unsigned kc = lds0 + L_KC + cw * (4 * KCS);
        cnt_add(cnt + C_PA + 4 * cw);
        cnt_wait(cnt + C_PA + 4 * cw, 2u);
        {
            const unsigned eo = kc + (rh ^ 1) * 4096u + lane * 16u;
#pragma unroll
            for (int i = 0; i < 4; ++i) lds_w128f(eo + i * 1024u, give[i]);
        }
        cnt_add(cnt + C_PB + 4 * cw);
        u64 uu[4];
        const int ty = 1024 * rh + 16 * a16 + 4 * kq;
#pragma unroll
        for (int i = 0; i < 4; ++i) uu[i] = lds_r64(lds0 + L_U + cw * 4608 + 512 + 2 * (ty + 256 * i));
        cnt_wait(cnt + C_PB + 4 * cw, 2u);
#pragma unroll
        for (int i = 0; i < 4; ++i) {
            const f32x4 y = lds_r128f(kc + rh * 4096u + lane * 16u + i * 1024u) + keep[i];
            const unsigned u0 = (unsigned)uu[i], u1 = (unsigned)(uu[i] >> 32);
            const u32x2 o = {cvtpk(fmaf(lo_f(u0), dch, y[0]), fmaf(hi_f(u0), dch, y[1])), cvtpk(fmaf(lo_f(u1), dch, y[2]), fmaf(hi_f(u1), dch, y[3]))};
            __builtin_amdgcn_raw_buffer_store_b64(o, wsr, WS_Y + (unsigned)((2 * cp + cw) * S_ + ty + 256 * i) * 2u, 0, 16);
        }
        WAITV(0);
        if (cnt_add_rtn(cnt + C_Y) == 3u && lane == 0) __hip_atomic_store((gu64*)(ws + WS_FY + (unsigned)cp * 8u), tag, RLX_AGENT);
        STAMP(8);
        const gu64* fy = (const gu64*)(ws + (lane < 16 ? WS_FY + (unsigned)(32 * C + 16 * cw + lane) * 8u : WS_FG + (unsigned)R * 8u));
        flag_wait(fy, ~tag, tag);
        STAMP(9);
        u32x4 yr[2];
#pragma unroll
        for (int i = 0; i < 2; ++i) yr[i] = bld128(wsr, WS_Y + (unsigned)((64 * C + 32 * cw + 16 * i + (lane >> 2)) * S_ + 64 * R + 32 * rh + 8 * pp) * 2u, 1);
        const float gt = __uint_as_float(__builtin_amdgcn_raw_buffer_load_b32(wsr, WS_G + (unsigned)tf * 4u, 0, 16));
        const unsigned stg = kc + 8192u + rh * 2048u;
#pragma unroll
        for (int i = 0; i < 2; ++i) lds_w128(stg + i * 1024u + lane * 16u, yr[i]);
        CFENCE();
#pragma unroll
        for (int g = 0; g < 4; ++g) {
            const u32x2 y = __builtin_bit_cast(u32x2, vtr(stg + (unsigned)(8 * g + 4 * h32 + q4) * 64u + blk * 32u + pp * 8u));
            lds_w128f(kc + rh * 4096u + (unsigned)(g * 64 + lane) * 16u,
                      f32x4{gt * lo_f(qv[g][0]) * lo_f(y[0]), gt * hi_f(qv[g][0]) * hi_f(y[0]), gt * lo_f(qv[g][1]) * lo_f(y[1]), gt * hi_f(qv[g][1]) * hi_f(y[1])});
        }
        lds_w32(kc + 12288u + rh * 256u + lane * 4u, __float_as_uint(1.f - gt));
        STAMP(10);
        LBAR();
        STAMP_OUT();
    }
}

extern "C" void kernel_launch(void* const* d_in, const int* in_sizes, int n_in, void* d_out, int out_size, void* d_ws, size_t ws_size,
                              hipStream_t stream) {
    Params p;
    p.x = (const u16*)d_in[0]; p.k = (const u16*)d_in[1]; p.v = (const u16*)d_in[2]; p.sk = (const u16*)d_in[3];
    p.sK = (const u16*)d_in[4]; p.sD = (const u16*)d_in[5]; p.w1 = (const u16*)d_in[7]; p.w2 = (const u16*)d_in[8];
    p.gw = (const u16*)d_in[9]; p.out = (u16*)d_out; p.ws = (char*)d_ws;
    (void)hipFuncSetAttribute((const void*)fused_kernel, hipFuncAttributeMaxDynamicSharedMemorySize, L_END);
    fused_kernel<<<256, 512, L_END, stream>>>(p);
}
```

```cpp
#include <hip/hip_runtime.h>
#include <stdint.h>

typedef unsigned short u16;
typedef unsigned long long u64;
using bf16x8 = __attribute__((ext_vector_type(8))) short;
using s16x4  = __attribute__((ext_vector_type(4))) short;
using f32x4  = __attribute__((ext_vector_type(4))) float;
using f32x16 = __attribute__((ext_vector_type(16))) float;
using u32x4  = __attribute__((ext_vector_type(4))) unsigned;
using u32x2  = __attribute__((ext_vector_type(2))) unsigned;
#define LAS __attribute__((address_space(3)))
#define GAS __attribute__((address_space(1)))
typedef GAS u64 gu64;
typedef LAS char* lptr;
typedef short v4i16_t __attribute__((ext_vector_type(4)));

constexpr int S_ = 2048, D_ = 512, F_ = 1792;
constexpr unsigned WS_EPOCH = 0;
constexpr unsigned WS_FKV = 4096;
constexpr unsigned WS_FH  = WS_FKV + 256 * 128;
constexpr unsigned WS_FY  = WS_FH + 256 * 128;
constexpr unsigned WS_G   = 256 * 1024;
constexpr unsigned WS_KVT = 1u << 20;
constexpr unsigned WS_Y   = 3u << 20;
constexpr unsigned WS_H   = 5u << 20;
constexpr unsigned WS_END = WS_H + 64u * 112u * 1024u;
constexpr unsigned L_RING = 0;
constexpr unsigned L_TR   = 131072;
constexpr unsigned L_W2   = 0;
constexpr unsigned L_KC   = 114688;
constexpr unsigned L_U    = 147968;
constexpr unsigned L_END  = 157184;
constexpr unsigned KCS = 4160;

#define RLX_AGENT __ATOMIC_RELAXED, __HIP_MEMORY_SCOPE_AGENT
#define SBAR() __builtin_amdgcn_sched_barrier(0)
#define MFMA32(a, b, c) __builtin_amdgcn_mfma_f32_32x32x16_bf16(a, b, c, 0, 0, 0)
#define MFMA16(a, b, c) __builtin_amdgcn_mfma_f32_16x16x32_bf16(a, b, c, 0, 0, 0)
#define DI __device__ __forceinline__

DI unsigned cvtpk(float lo, float hi) { unsigned r; asm("v_cvt_pk_bf16_f32 %0, %1, %2" : "=v"(r) : "v"(lo), "v"(hi)); return r; }
DI float lo_f(unsigned x) { return __uint_as_float(x << 16); }
DI float hi_f(unsigned x) { return __uint_as_float(x & 0xffff0000u); }
DI void glds16(unsigned voff, const void* sbase, unsigned lds_base) {
    unsigned sv;
    asm volatile("s_mov_b32 %0, m0\n\ts_mov_b32 m0, %3\n\ts_nop 0\n\tglobal_load_lds_dwordx4 %1, %2\n\ts_mov_b32 m0, %0"
                 : "=&s"(sv) : "v"(voff), "s"(sbase), "s"(lds_base) : "memory");
}
DI s16x4 vtr(unsigned a) { return __builtin_bit_cast(s16x4, __builtin_amdgcn_ds_read_tr16_b64_v4i16((LAS v4i16_t*)(uintptr_t)a)); }
DI bf16x8 cat8(s16x4 a, s16x4 b) { return __builtin_shufflevector(a, b, 0, 1, 2, 3, 4, 5, 6, 7); }
DI bf16x8 lds_r128(unsigned a) { return *(const LAS bf16x8*)(uintptr_t)a; }
DI u32x4 lds_r128u(unsigned a) { return *(const LAS u32x4*)(uintptr_t)a; }
DI f32x4 lds_r128f(unsigned a) { return *(const LAS f32x4*)(uintptr_t)a; }
DI void lds_w128(unsigned a, u32x4 v) { *(LAS u32x4*)(uintptr_t)a = v; }
DI void lds_w128f(unsigned a, f32x4 v) { *(LAS f32x4*)(uintptr_t)a = v; }
DI void lds_w32(unsigned a, unsigned v) { *(LAS unsigned*)(uintptr_t)a = v; }
DI u64 lds_r64(unsigned a) { return __hip_atomic_load((LAS u64*)(uintptr_t)a, __ATOMIC_RELAXED, __HIP_MEMORY_SCOPE_WAVEFRONT); }

#define GLD16(dst, ptr) asm volatile("global_load_dwordx4 %0, %1, off" : "=v"(dst) : "v"(ptr) : "memory")
#define GLD8(dst, ptr)  asm volatile("global_load_dwordx2 %0, %1, off" : "=v"(dst) : "v"(ptr) : "memory")
#define WAITV(n) asm volatile("s_waitcnt vmcnt(" #n ")" ::: "memory")
#define WAIT_BAR(n) asm volatile("s_waitcnt vmcnt(" #n ") lgkmcnt(0)\n\ts_barrier" ::: "memory")
#define LBAR() asm volatile("s_waitcnt lgkmcnt(0)\n\ts_barrier" ::: "memory")

constexpr unsigned SPIN_CAP = 1u << 20;

struct Params {
    const u16* x; const u16* k; const u16* v; const u16* sk; const u16* sK; const u16* sD;
    const u16* w1; const u16* w2; const u16* gw; u16* out; char* ws;
};

DI void wait_flags(const char* base, unsigned stride, int n, u64 tag, int lane) {
    const gu64* p = (const gu64*)(base + (unsigned)(lane < n ? lane : 0) * stride);
    for (unsigned spins = 0; spins < SPIN_CAP; ++spins) {
        u64 f = __hip_atomic_load(p, RLX_AGENT);
        if (__all(f == tag)) break;
        __builtin_amdgcn_s_sleep(1);
    }
    __builtin_amdgcn_fence(__ATOMIC_ACQUIRE, "wavefront");
}

__global__ void __launch_bounds__(256, 1) fused_kernel(Params p) {
    extern __shared__ __attribute__((aligned(16))) char smem[];
    const unsigned lds0 = (unsigned)(uintptr_t)(LAS char*)smem;
    const int tid = threadIdx.x, lane = tid & 63;
    const int w = __builtin_amdgcn_readfirstlane(tid >> 6);
    const int b = blockIdx.x, xcd = b & 7, j = b >> 3;
    const int R = xcd * 4 + (j >> 3), C = j & 7;
    const bool gemm1 = j < 28;
    const int mt = (xcd >> 1) * 4 + j / 7, nt = (xcd & 1) * 7 + j % 7;
    const int cp = b;
    char* ws = p.ws;
    const __amdgpu_buffer_rsrc_t wsr = __builtin_amdgcn_make_buffer_rsrc(ws, 0, WS_END, 0x00020000);

    u64 ep;
    asm volatile("global_load_dwordx2 %0, %1, %2 sc1" : "=v"(ep) : "v"(0u), "s"(ws) : "memory");

    u32x4 kr[5], vr[2], sr[4], kA[2]; u32x2 kB[2];
    {
        const int tp = tid >> 3, cc = tid & 7, t0 = 64 * R + 2 * tp, ch = 64 * C + 8 * cc;
#pragma unroll
        for (int i = 0; i < 5; ++i) { int t = t0 - 3 + i; t = t < 0 ? 0 : t; GLD16(kr[i], p.k + t * D_ + ch); }
#pragma unroll
        for (int i = 0; i < 2; ++i) GLD16(vr[i], p.v + (t0 + i) * D_ + ch);
#pragma unroll
        for (int i = 0; i < 4; ++i) GLD16(sr[i], p.sk + ch * 4 + i * 8);
#pragma unroll
        for (int h = 0; h < 2; ++h) {
            const u16* kp = p.sK + (2 * cp + h) * S_;
            GLD16(kA[h], kp + 2040 - 8 * tid);
            GLD8(kB[h], kp + (tid == 255 ? 0 : 2036 - 8 * tid));
        }
    }
    const unsigned xoffE = (unsigned)(lane >> 3) * 1024u + (unsigned)(((lane & 7) ^ ((lane >> 4) & 7)) * 16);
    const unsigned xoffO = (unsigned)(lane >> 3) * 1024u + (unsigned)(((lane & 7) ^ (((lane >> 4) + 4) & 7)) * 16);
    const unsigned woff = (unsigned)(lane >> 4) * 3584u + (unsigned)((((lane & 15) ^ ((lane >> 4) << 2))) * 16);
    const char* xg = (const char*)p.x + (size_t)(128 * mt + 32 * w) * 1024;
    const char* wg = (const char*)p.w1 + (size_t)(16 * w) * 3584 + nt * 256;
#define G1_STAGE(s) do { const unsigned lb = lds0 + L_RING + ((s) & 3) * 32768u + w * 4096u;                 \
        _Pragma("unroll") for (int ii = 0; ii < 4; ++ii) glds16((ii & 1) ? xoffO : xoffE, xg + ii * 8192 + (s) * 128, lb + ii * 1024);          \
        _Pragma("unroll") for (int ii = 0; ii < 4; ++ii) glds16(woff ^ (ii * 16), wg + (size_t)((s) * 64 + ii * 4) * 3584, lb + 16384 + ii * 1024); } while (0)
    if (gemm1) {
#pragma unroll
        for (int s = 0; s < 4; ++s) G1_STAGE(s);
    }
    asm volatile("s_cmp_gt_i32 %[jj], 27\n\ts_cbranch_scc0 .Lw%=\n\ts_waitcnt vmcnt(0)\n.Lw%=:\n\ts_waitcnt vmcnt(32)"
                 : "+v"(kr[0]), "+v"(kr[1]), "+v"(kr[2]), "+v"(kr[3]), "+v"(kr[4]), "+v"(vr[0]), "+v"(vr[1]),
                   "+v"(sr[0]), "+v"(sr[1]), "+v"(sr[2]), "+v"(sr[3]), "+v"(kA[0]), "+v"(kA[1]), "+v"(kB[0]), "+v"(kB[1]), "+v"(ep)
                 : [jj] "s"(j) : "memory", "scc");
    const u64 tag = ep + 0x9E3779B97F4A7C15ull;
    {
        const int tp = tid >> 3, cc = tid & 7, t0 = 64 * R + 2 * tp;
#pragma unroll
        for (int i = 0; i < 3; ++i) if (t0 - 3 + i < 0) kr[i] = u32x4{0u, 0u, 0u, 0u};
#pragma unroll
        for (int e = 0; e < 8; ++e) {
            const unsigned s01 = sr[e >> 1][(e & 1) * 2], s23 = sr[e >> 1][(e & 1) * 2 + 1];
            const float s0 = lo_f(s01), s1 = hi_f(s01), s2 = lo_f(s23), s3 = hi_f(s23);
            float kk[5];
#pragma unroll
            for (int i = 0; i < 5; ++i) kk[i] = (e & 1) ? hi_f(kr[i][e >> 1]) : lo_f(kr[i][e >> 1]);
            const float va = (e & 1) ? hi_f(vr[0][e >> 1]) : lo_f(vr[0][e >> 1]);
            const float vb = (e & 1) ? hi_f(vr[1][e >> 1]) : lo_f(vr[1][e >> 1]);
            unsigned acc = cvtpk(kk[0] * s3, kk[1] * s3);
            unsigned pr = cvtpk(kk[1] * s2, kk[2] * s2);
            acc = cvtpk(lo_f(acc) + lo_f(pr), hi_f(acc) + hi_f(pr));
            pr = cvtpk(kk[2] * s1, kk[3] * s1);
            acc = cvtpk(lo_f(acc) + lo_f(pr), hi_f(acc) + hi_f(pr));
            pr = cvtpk(kk[3] * s0, kk[4] * s0);
            acc = cvtpk(lo_f(acc) + lo_f(pr), hi_f(acc) + hi_f(pr));
            const unsigned kv = cvtpk(lo_f(acc) * va, hi_f(acc) * vb);
            lds_w32(lds0 + L_TR + (unsigned)((8 * cc + e) * 32 + (tp ^ (cc << 2))) * 4, kv);
        }
        LBAR();
#pragma unroll
        for (int i = 0; i < 2; ++i) {
            const int row = (tid >> 3) + 32 * i, q = tid & 7;
            const u32x4 vv = lds_r128u(lds0 + L_TR + (unsigned)(row * 128 + ((q ^ (row >> 3)) * 16)));
            __builtin_amdgcn_raw_buffer_store_b128(vv, wsr, WS_KVT + (unsigned)((64 * C + row) * S_ + 64 * R + 8 * q) * 2, 0, 16);
        }
    }
    gu64* fkv = (gu64*)(ws + WS_FKV + (unsigned)(C * 32 + R) * 128);
    if (gemm1) {
        const int wn = w >> 1, wm = w & 1, h = lane >> 5, blk = (lane >> 4) & 1, q = (lane & 15) >> 2, pp = lane & 3;
        unsigned aaddr[2][2], baddr[4];
#pragma unroll
        for (int a = 0; a < 2; ++a)
#pragma unroll
            for (int t = 0; t < 2; ++t) {
                const int row = 8 * h + 4 * t + q, ch = 4 * (2 * wn + a) + 2 * blk + (pp >> 1);
                aaddr[a][t] = lds0 + L_RING + 16384 + 256 * row + 16 * (ch ^ ((q << 2) | (2 * h + t))) + 8 * (pp & 1);
            }
#pragma unroll
        for (int ks = 0; ks < 4; ++ks) {
            const int row = 64 * wm + (lane & 31);
            baddr[ks] = lds0 + L_RING + row * 128 + 16 * ((2 * ks + h) ^ ((row >> 1) & 7));
        }
        f32x16 acc[2][2];
#pragma unroll
        for (int a = 0; a < 2; ++a)
#pragma unroll
            for (int c = 0; c < 2; ++c) acc[a][c] = f32x16{};
        bf16x8 af[2][2], bfr[2][2];
#define G1_READ(buf, so, ks) do { _Pragma("unroll") for (int a = 0; a < 2; ++a) af[buf][a] = cat8(vtr(aaddr[a][0] + (so) + (ks) * 4096), vtr(aaddr[a][1] + (so) + (ks) * 4096)); \
        _Pragma("unroll") for (int c = 0; c < 2; ++c) bfr[buf][c] = lds_r128(baddr[ks] + (so) + c * 4096); } while (0)
#define G1_MMA(buf) do { _Pragma("unroll") for (int a = 0; a < 2; ++a) _Pragma("unroll") for (int c = 0; c < 2; ++c) acc[a][c] = MFMA32(af[buf][a], bfr[buf][c], acc[a][c]); } while (0)
        WAIT_BAR(26);
        G1_READ(0, 0u, 0);
#pragma unroll
        for (int s = 0; s < 8; ++s) {
            const unsigned so = (s & 3) * 32768u, sn = ((s + 1) & 3) * 32768u;
            G1_READ(1, so, 1); SBAR(); G1_MMA(0); SBAR();
            G1_READ(0, so, 2); SBAR(); G1_MMA(1); SBAR();
            G1_READ(1, so, 3); SBAR(); G1_MMA(0); SBAR();
            if (s == 0) WAIT_BAR(18); else if (s == 1) WAIT_BAR(18); else if (s == 2) WAIT_BAR(18);
            else if (s == 3) WAIT_BAR(16); else if (s == 4) WAIT_BAR(16); else if (s == 5) WAIT_BAR(8); else WAIT_BAR(0);
            if (s == 3 && tid == 0) __hip_atomic_store(fkv, tag, RLX_AGENT);
            if (s < 4) G1_STAGE(s + 4);
            if (s < 7) G1_READ(0, sn, 0);
            SBAR(); G1_MMA(1); SBAR();
        }
#pragma unroll
        for (int a = 0; a < 2; ++a)
#pragma unroll
            for (int c = 0; c < 2; ++c)
#pragma unroll
                for (int s = 0; s < 2; ++s) {
                    u32x4 o;
#pragma unroll
                    for (int i = 0; i < 4; ++i) o[i] = cvtpk(fmaxf(acc[a][c][8 * s + 2 * i], 0.f), fmaxf(acc[a][c][8 * s + 2 * i + 1], 0.f));
                    const unsigned mb = 4 * mt + 2 * wm + c, ksg = 8 * nt + 4 * wn + 2 * a + s;
                    __builtin_amdgcn_raw_buffer_store_b128(o, wsr, WS_H + (mb * 112u + ksg) * 1024u + lane * 16u, 0, 16);
                }
    } else {
        WAIT_BAR(0);
        if (tid == 0) __hip_atomic_store(fkv, tag, RLX_AGENT);
        const int sp = xcd * 4 + (j - 28);
        const u32x4 gwv = *(const u32x4*)(p.gw + lane * 8);
        u32x4 xr[16];
#pragma unroll
        for (int i = 0; i < 16; ++i) xr[i] = *(const u32x4*)(p.x + (64 * sp + 16 * w + i) * D_ + lane * 8);
        float myg = 0.f;
#pragma unroll
        for (int i = 0; i < 16; ++i) {
            float s = 0.f;
#pragma unroll
            for (int e = 0; e < 4; ++e) { const unsigned pr = cvtpk(lo_f(xr[i][e]) * lo_f(gwv[e]), hi_f(xr[i][e]) * hi_f(gwv[e])); s += lo_f(pr) + hi_f(pr); }
#pragma unroll
            for (int o = 32; o >= 1; o >>= 1) s += __shfl_xor(s, o);
            if (lane == i) myg = s;
        }
        const float gs = lo_f(cvtpk(myg, 0.f));
        const float g = lo_f(cvtpk(1.f / (1.f + __expf(-gs)), 0.f));
        if (lane < 16) __hip_atomic_store((GAS float*)(ws + WS_G) + 64 * sp + 16 * w + lane, g, RLX_AGENT);
    }
    {
#pragma unroll
        for (int h = 0; h < 2; ++h) {
            unsigned W[6] = {tid == 255 ? 0u : kB[h][0], tid == 255 ? 0u : kB[h][1], kA[h][0], kA[h][1], kA[h][2], kA[h][3]};
            unsigned Rr[6], Mm[6];
#pragma unroll
            for (int i = 1; i < 6; ++i) { Rr[i] = __builtin_amdgcn_alignbit(W[i], W[i], 16); Mm[i] = (W[i] & 0xffffu) | (W[i - 1] & 0xffff0000u); }
            const unsigned kb = lds0 + L_KC + h * 4 * KCS + tid * 16;
            lds_w128(kb + 0 * KCS, u32x4{Rr[5], Rr[4], Rr[3], Rr[2]});
            lds_w128(kb + 1 * KCS, u32x4{Mm[5], Mm[4], Mm[3], Mm[2]});
            lds_w128(kb + 2 * KCS, u32x4{Rr[4], Rr[3], Rr[2], Rr[1]});
            lds_w128(kb + 3 * KCS, u32x4{Mm[4], Mm[3], Mm[2], Mm[1]});
        }
        if (tid < 16) lds_w128(lds0 + L_KC + (tid >> 1) * KCS + 4096 + (tid & 1) * 16, u32x4{0u, 0u, 0u, 0u});
        if (tid < 64) lds_w128(lds0 + L_U + (tid >> 5) * 4608 + (tid & 31) * 16, u32x4{0u, 0u, 0u, 0u});
    }
    WAIT_BAR(0);
    if (tid == 0) __hip_atomic_store((gu64*)(ws + WS_FH + (unsigned)(gemm1 ? mt * 14 + nt : 224 + xcd * 4 + (j - 28)) * 128), tag, RLX_AGENT);
    wait_flags(ws + WS_FKV + (unsigned)((cp >> 5) * 32 + 8 * w) * 128, 128, 8, tag, lane);
    u32x4 ur[2];
#pragma unroll
    for (int h = 0; h < 2; ++h)
        asm volatile("buffer_load_dwordx4 %0, %1, %2, 0 offen sc1" : "=v"(ur[h]) : "v"(WS_KVT + (unsigned)((2 * cp + h) * S_ + 512 * w + 8 * lane) * 2), "s"(wsr) : "memory");
    const unsigned w2off = (unsigned)(lane >> 3) * 1024u + (unsigned)(((lane & 7) ^ (((lane >> 4) & 1) << 2)) * 16);
    const char* w2g = (const char*)p.w2 + (size_t)(448 * w) * 1024 + C * 128;
    const unsigned w2l = lds0 + L_W2 + w * 28672u;
#define W2_CHUNK(c) do { _Pragma("unroll") for (int ii = 0; ii < 7; ++ii) glds16(w2off, w2g + (size_t)((c) * 56 + ii * 8) * 1024, w2l + ((c) & 3) * 7168u + ii * 1024u); } while (0)
#pragma unroll
    for (int c = 0; c < 4; ++c) W2_CHUNK(c);
    asm volatile("s_waitcnt vmcnt(28)" : "+v"(ur[0]), "+v"(ur[1]) :: "memory");
#pragma unroll
    for (int h = 0; h < 2; ++h) lds_w128(lds0 + L_U + h * 4608 + 512 + (512 * w + 8 * lane) * 2, ur[h]);
    LBAR();
    bf16x8 w2f[14][2];
    f32x4 cacc[8];
    const int h32 = lane >> 5, blk = (lane >> 4) & 1, q4 = (lane & 15) >> 2, pp = lane & 3;
    const unsigned w2a0 = w2l + 128 * (4 * h32 + q4) + 16 * ((2 * blk + (pp >> 1)) ^ ((q4 >> 1) << 2)) + 8 * (pp & 1);
    {
        const int cw = w >> 1, rh = w & 1, a16 = lane & 15, kq = lane >> 4;
        const unsigned kaddr = lds0 + L_KC + cw * 4 * KCS + (3 - (a16 & 3)) * KCS + 2 * (2052 - 4 * (a16 >> 2) - 8 * kq) - 64 * 63;
        const unsigned uaddr = lds0 + L_U + cw * 4608 + 2 * (256 + 16 * a16 + 8 - 8 * kq) - 448;
#pragma unroll
        for (int qq = 0; qq < 8; ++qq) cacc[qq] = f32x4{0.f, 0.f, 0.f, 0.f};
#pragma unroll
        for (int rr = 0; rr < 4; ++rr) {
            const int r = 4 * rh + rr;
            bf16x8 A[8], B[8];
#pragma unroll
            for (int e = 0; e < 8; ++e) {
                const unsigned ka = kaddr + 64 * (63 - 8 * e) - 64 * r;
                const u64 x0 = lds_r64(ka), x1 = lds_r64(ka + 8);
                A[e] = __builtin_bit_cast(bf16x8, u32x4{(unsigned)x0, (unsigned)(x0 >> 32), (unsigned)x1, (unsigned)(x1 >> 32)});
                B[e] = lds_r128(uaddr + 512 * e + 64 * (7 - r));
            }
#pragma unroll
            for (int e = 0; e < 8; ++e)
#pragma unroll
                for (int d = 0; d < 8 - e; ++d) cacc[e + d] = MFMA16(A[e], B[d], cacc[e + d]);
            WAITV(21);
#pragma unroll
            for (int hh = 0; hh < 7; ++hh) {
                const int hs = 7 * rr + hh;
#pragma unroll
                for (int a = 0; a < 2; ++a) {
                    const s16x4 v = vtr((w2a0 ^ (a * 64)) + rr * 7168u + hh * 1024u);
                    if (hs & 1) { w2f[hs >> 1][a][4] = v[0]; w2f[hs >> 1][a][5] = v[1]; w2f[hs >> 1][a][6] = v[2]; w2f[hs >> 1][a][7] = v[3]; }
                    else        { w2f[hs >> 1][a][0] = v[0]; w2f[hs >> 1][a][1] = v[1]; w2f[hs >> 1][a][2] = v[2]; w2f[hs >> 1][a][3] = v[3]; }
                }
            }
            asm volatile("s_waitcnt lgkmcnt(0)" ::: "memory");
            W2_CHUNK(rr + 4);
        }
    }
    LBAR();
    {
        const unsigned rb = lds0 + L_KC;
#pragma unroll
        for (int qq = 0; qq < 8; ++qq) lds_w128f(rb + (unsigned)((w * 8 + qq) * 64 + lane) * 16, cacc[qq]);
        LBAR();
        const unsigned dd = *(const unsigned*)(p.sD + 2 * cp);
        const float d0 = lo_f(dd), d1 = hi_f(dd);
#pragma unroll
        for (int i = 0; i < 2; ++i) {
            const int qq = 2 * w + i;
            const f32x4 y0 = lds_r128f(rb + (unsigned)((0 * 8 + qq) * 64 + lane) * 16) + lds_r128f(rb + (unsigned)((1 * 8 + qq) * 64 + lane) * 16);
            const f32x4 y1 = lds_r128f(rb + (unsigned)((2 * 8 + qq) * 64 + lane) * 16) + lds_r128f(rb + (unsigned)((3 * 8 + qq) * 64 + lane) * 16);
            const int t = 256 * qq + 16 * (lane & 15) + 4 * (lane >> 4);
            const u64 u0 = lds_r64(lds0 + L_U + 512 + 2 * t), u1 = lds_r64(lds0 + L_U + 4608 + 512 + 2 * t);
            u32x4 o;
#pragma unroll
            for (int e = 0; e < 4; ++e) {
                const unsigned a0 = (unsigned)(u0 >> (32 * (e >> 1))), a1 = (unsigned)(u1 >> (32 * (e >> 1)));
                const float ua = (e & 1) ? hi_f(a0) : lo_f(a0), ub = (e & 1) ? hi_f(a1) : lo_f(a1);
                const unsigned yy = cvtpk(y0[e], y1[e]), ud = cvtpk(ua * d0, ub * d1);
                o[e] = cvtpk(lo_f(yy) + lo_f(ud), hi_f(yy) + hi_f(ud));
            }
            __builtin_amdgcn_raw_buffer_store_b128(o, wsr, WS_Y + (unsigned)(cp * S_ + t) * 4, 0, 16);
        }
    }
    WAITV(0);
    if (lane == 0) __hip_atomic_store((gu64*)(ws + WS_FY + (unsigned)(cp * 4 + w) * 64), tag, RLX_AGENT);
    {
        const int n_lo = (448 * w) >> 7, n_hi = (448 * w + 447) >> 7;
        wait_flags(ws + WS_FH + (unsigned)((R >> 1) * 14 + n_lo) * 128, 128, n_hi - n_lo + 1, tag, lane);
    }
    f32x16 acc[2][2];
#pragma unroll
    for (int a = 0; a < 2; ++a)
#pragma unroll
        for (int c = 0; c < 2; ++c) acc[a][c] = f32x16{};
    {
        bf16x8 hf[28][2];
#pragma unroll
        for (int ks = 0; ks < 28; ++ks)
#pragma unroll
            for (int c = 0; c < 2; ++c)
                hf[ks][c] = __builtin_bit_cast(bf16x8, __builtin_amdgcn_raw_buffer_load_b128(wsr, WS_H + ((2 * R + c) * 112u + 28 * w + ks) * 1024u + lane * 16u, 0, 16));
        SBAR();
#pragma unroll
        for (int ks = 0; ks < 28; ++ks) {
            bf16x8 wf[2];
#pragma unroll
            for (int a = 0; a < 2; ++a) {
                if (ks < 14) wf[a] = w2f[ks][a];
                else wf[a] = cat8(vtr((w2a0 ^ (a * 64)) + (ks - 14) * 2048u), vtr((w2a0 ^ (a * 64)) + (ks - 14) * 2048u + 1024u));
            }
#pragma unroll
            for (int a = 0; a < 2; ++a)
#pragma unroll
                for (int c = 0; c < 2; ++c) acc[a][c] = MFMA32(wf[a], hf[ks][c], acc[a][c]);
        }
    }
    {
#pragma unroll
        for (int tl = 0; tl < 4; ++tl)
            if (tl != w) {
#pragma unroll
                for (int g = 0; g < 4; ++g) {
                    const f32x16 v = acc[tl >> 1][tl & 1];
                    lds_w128f(w2l + (unsigned)((tl * 4 + g) * 64 + lane) * 16, f32x4{v[4 * g], v[4 * g + 1], v[4 * g + 2], v[4 * g + 3]});
                }
            }
    }
    const int oa = w >> 1, oc = w & 1;
    const int t = 64 * R + 32 * oc + (lane & 31), chb = 64 * C + 32 * oa + 4 * h32;
    wait_flags(ws + WS_FY + (unsigned)((32 * C + 16 * oa) * 4 + (R >> 3)) * 64, 256, 16, tag, lane);
    wait_flags(ws + WS_FH + (unsigned)(224 + R) * 128, 128, 1, tag, lane);
    unsigned yv[4][2]; u32x2 qv[4];
#pragma unroll
    for (int g = 0; g < 4; ++g) {
#pragma unroll
        for (int i = 0; i < 2; ++i) yv[g][i] = __builtin_amdgcn_raw_buffer_load_b32(wsr, WS_Y + (unsigned)((((chb + 8 * g) >> 1) + i) * S_ + t) * 4, 0, 16);
        qv[g] = *(const u32x2*)(p.x + t * D_ + chb + 8 * g);
    }
    const float gt = __uint_as_float(__builtin_amdgcn_raw_buffer_load_b32(wsr, WS_G + (unsigned)t * 4, 0, 16));
    LBAR();
    {
        f32x16 own = (w == 0) ? acc[0][0] : (w == 1) ? acc[0][1] : (w == 2) ? acc[1][0] : acc[1][1];
        const float og = lo_f(cvtpk(1.f - gt, 0.f));
#pragma unroll
        for (int g = 0; g < 4; ++g) {
            f32x4 s = f32x4{own[4 * g], own[4 * g + 1], own[4 * g + 2], own[4 * g + 3]};
#pragma unroll
            for (int o = 1; o < 4; ++o) {
                const int sw = (w + o) & 3;
                s += lds_r128f(lds0 + L_W2 + sw * 28672u + (unsigned)((w * 4 + g) * 64 + lane) * 16);
            }
            u32x2 o2;
#pragma unroll
            for (int i = 0; i < 2; ++i) {
                const unsigned bb = cvtpk(s[2 * i], s[2 * i + 1]);
                const unsigned aa = cvtpk(lo_f(qv[g][i]) * lo_f(yv[g][i]), hi_f(qv[g][i]) * hi_f(yv[g][i]));
                const unsigned ga = cvtpk(gt * lo_f(aa), gt * hi_f(aa));
                const unsigned gb = cvtpk(og * lo_f(bb), og * hi_f(bb));
                o2[i] = cvtpk(lo_f(ga) + lo_f(gb), hi_f(ga) + hi_f(gb));
            }
            *(u32x2*)(p.out + t * D_ + chb + 8 * g) = o2;
        }
    }
    if (b == 0 && w == 0) {
#pragma unroll 1
        for (int i = 0; i < 4; ++i) wait_flags(ws + WS_FY + (unsigned)(i * 64) * 256, 256, 64, tag, lane);
        if (lane == 0) __hip_atomic_store((gu64*)(ws + WS_EPOCH), tag, RLX_AGENT);
    }
}

extern "C" void kernel_launch(void* const* d_in, const int* in_sizes, int n_in, void* d_out, int out_size, void* d_ws, size_t ws_size,
                              hipStream_t stream) {
    Params p;
    p.x = (const u16*)d_in[0]; p.k = (const u16*)d_in[1]; p.v = (const u16*)d_in[2]; p.sk = (const u16*)d_in[3];
    p.sK = (const u16*)d_in[4]; p.sD = (const u16*)d_in[5]; p.w1 = (const u16*)d_in[7]; p.w2 = (const u16*)d_in[8];
    p.gw = (const u16*)d_in[9]; p.out = (u16*)d_out; p.ws = (char*)d_ws;
    hipFuncSetAttribute((const void*)fused_kernel, hipFuncAttributeMaxDynamicSharedMemorySize, L_END);
    fused_kernel<<<256, 256, L_END, stream>>>(p);
}
```

```cpp
#include <hip/hip_runtime.h>
#include <stdint.h>

typedef unsigned short u16;
typedef unsigned long long u64;
using bf16x8 = __attribute__((ext_vector_type(8))) short;
using s16x4  = __attribute__((ext_vector_type(4))) short;
using s16x2  = __attribute__((ext_vector_type(2))) short;
using f32x2  = __attribute__((ext_vector_type(2))) float;
using f32x4  = __attribute__((ext_vector_type(4))) float;
using f32x16 = __attribute__((ext_vector_type(16))) float;
using u32x4  = __attribute__((ext_vector_type(4))) unsigned;
using u32x2  = __attribute__((ext_vector_type(2))) unsigned;
using bf2    = __attribute__((ext_vector_type(2))) __bf16;
#define LAS __attribute__((address_space(3)))
#define GAS __attribute__((address_space(1)))
typedef GAS u64 gu64;
typedef short v4i16_t __attribute__((ext_vector_type(4)));
typedef __amdgpu_buffer_rsrc_t rsrc_t;

constexpr int S_ = 2048, D_ = 512, F_ = 1792;
constexpr unsigned WS_EPOCH = 0;
constexpr unsigned WS_FKV = 4096;
constexpr unsigned WS_FH  = 8192;
constexpr unsigned WS_FY  = 12288;
constexpr unsigned WS_FG  = 16384;
constexpr unsigned WS_G   = 32768;
constexpr unsigned WS_KVT = 1u << 20;
constexpr unsigned WS_Y   = 3u << 20;
constexpr unsigned WS_H   = 5u << 20;
constexpr unsigned WS_END = WS_H + 64u * 112u * 1024u;
constexpr unsigned L_KC   = 131072;
constexpr unsigned L_U    = 147712;
constexpr unsigned L_CNT  = 156928;
constexpr unsigned L_END  = 157056;
constexpr unsigned KCS = 4160;
#define SLOT(s) ((unsigned)((((s) + 3) & 3) * 32768))
enum { C_PROG = 0, C_H = 16, C_TR = 20, C_KV = 24, C_U = 28, C_PA = 32, C_PB = 40, C_Y = 48, C_TAG = 56, C_HM = 64, C_KM = 68, C_YM = 72 };

#define RLX_AGENT __ATOMIC_RELAXED, __HIP_MEMORY_SCOPE_AGENT
#define SBAR() __builtin_amdgcn_sched_barrier(0)
#define CFENCE() asm volatile("" ::: "memory")
#define MFMA32(a, b, c) __builtin_amdgcn_mfma_f32_32x32x16_bf16(a, b, c, 0, 0, 0)
#define MFMA16(a, b, c) __builtin_amdgcn_mfma_f32_16x16x32_bf16(a, b, c, 0, 0, 0)
#define DI __device__ __forceinline__

DI unsigned cvtpk(float lo, float hi) { return __builtin_bit_cast(unsigned, __builtin_convertvector(f32x2{lo, hi}, bf2)); }
DI float lo_f(unsigned x) { return __uint_as_float(x << 16); }
DI float hi_f(unsigned x) { return __uint_as_float(x & 0xffff0000u); }
DI float dot2(unsigned a, unsigned b, float c) { return __builtin_amdgcn_fdot2_f32_bf16(__builtin_bit_cast(bf2, a), __builtin_bit_cast(bf2, b), c, false); }
template <unsigned LO, unsigned SO> DI void dma(unsigned voff, rsrc_t r, unsigned sb, unsigned lb) {
    unsigned t;
    asm volatile("s_add_u32 m0, %4, %5\n\ts_add_u32 %0, %3, %6\n\tbuffer_load_dwordx4 %1, %2, %0 offen lds"
                 : "=&s"(t) : "v"(voff), "s"(r), "s"(sb), "s"(lb), "n"(LO), "n"(SO) : "memory", "scc");
}
DI s16x4 vtr(unsigned a) { return __builtin_bit_cast(s16x4, __builtin_amdgcn_ds_read_tr16_b64_v4i16((LAS v4i16_t*)(uintptr_t)a)); }
DI bf16x8 cat8(s16x4 a, s16x4 b) { return __builtin_shufflevector(a, b, 0, 1, 2, 3, 4, 5, 6, 7); }
DI bf16x8 lds_r128(unsigned a) { return *(const LAS bf16x8*)(uintptr_t)a; }
DI u32x4 lds_r128u(unsigned a) { return *(const LAS u32x4*)(uintptr_t)a; }
DI f32x4 lds_r128f(unsigned a) { return *(const LAS f32x4*)(uintptr_t)a; }
DI unsigned lds_r32(unsigned a) { return *(const LAS unsigned*)(uintptr_t)a; }
DI void lds_w128(unsigned a, u32x4 v) { *(LAS u32x4*)(uintptr_t)a = v; }
DI void lds_w128f(unsigned a, f32x4 v) { *(LAS f32x4*)(uintptr_t)a = v; }
DI void lds_w32(unsigned a, unsigned v) { *(LAS unsigned*)(uintptr_t)a = v; }
DI u64 lds_r64(unsigned a) { return __hip_atomic_load((LAS u64*)(uintptr_t)a, __ATOMIC_RELAXED, __HIP_MEMORY_SCOPE_WAVEFRONT); }
DI u32x4 bld128(rsrc_t r, unsigned vo, int aux) { return aux ? __builtin_amdgcn_raw_buffer_load_b128(r, vo, 0, 16) : __builtin_amdgcn_raw_buffer_load_b128(r, vo, 0, 0); }

#define WAITV(n) asm volatile("s_waitcnt vmcnt(" #n ")" ::: "memory")
#define WAITL() asm volatile("s_waitcnt lgkmcnt(0)" ::: "memory")
#define LBAR() asm volatile("s_waitcnt lgkmcnt(0)\n\ts_barrier" ::: "memory")

constexpr unsigned SPIN_CAP = 1u << 18;

DI void cnt_add(unsigned a) {
    asm volatile("s_mov_b64 exec, 1\n\tds_add_u32 %0, %1\n\ts_mov_b64 exec, -1" :: "v"(a), "v"(1u) : "memory");
}
DI unsigned cnt_add_rtn(unsigned a) {
    unsigned r;
    asm volatile("s_mov_b64 exec, 1\n\tds_add_rtn_u32 %0, %1, %2\n\ts_mov_b64 exec, -1\n\ts_waitcnt lgkmcnt(0)" : "=&v"(r) : "v"(a), "v"(1u) : "memory");
    return __builtin_amdgcn_readfirstlane(r);
}
DI void cnt_set(unsigned a, unsigned v) {
    asm volatile("s_mov_b64 exec, 1\n\tds_write_b32 %0, %1\n\ts_mov_b64 exec, -1" :: "v"(a), "v"(v) : "memory");
}
DI void cnt_wait(unsigned a, unsigned n) {
#pragma unroll 1
    for (unsigned spins = 0; spins < SPIN_CAP * 8; ++spins) {
        CFENCE();
        if (__builtin_amdgcn_readfirstlane(lds_r32(a)) >= n) break;
    }
    CFENCE();
}
DI unsigned min4(u32x4 v) { return __builtin_amdgcn_readfirstlane(min(min(v[0], v[1]), min(v[2], v[3]))); }
DI void prog_wait(unsigned a, unsigned n) {
#pragma unroll 1
    for (unsigned spins = 0; spins < SPIN_CAP * 8; ++spins) {
        CFENCE();
        if (min4(lds_r128u(a)) >= n) break;
    }
    CFENCE();
}
DI void mask_wait(unsigned a, unsigned m) {
#pragma unroll 1
    for (unsigned spins = 0; spins < SPIN_CAP * 8; ++spins) {
        CFENCE();
        if ((__builtin_amdgcn_readfirstlane(lds_r32(a)) & m) == m) break;
        __builtin_amdgcn_s_sleep(1);
    }
    CFENCE();
}
#define PSL 10
#define PSF 24
#define PQ 48
#define PSK 20
#define PSH0 32
#define PSS 24
#define PSH 10
#define PSY 40
DI void flag_wait(const gu64* p, u64 tag) {
#pragma unroll 1
    for (unsigned spins = 0; spins < SPIN_CAP; ++spins) {
        const u64 m = __ballot(__hip_atomic_load(p, RLX_AGENT) == tag);
        if (m == ~0ull) break;
        if (__popcll(m) < PQ) __builtin_amdgcn_s_sleep(PSF); else __builtin_amdgcn_s_sleep(PSL);
    }
    CFENCE();
}

constexpr int LEAD = 8;
constexpr int hslot(int n) {
    return n >= 56 ? 0 : n < 2 * LEAD ? 24 + n : ((n - 2 * LEAD) & 3) == 0 ? hslot((n - 2 * LEAD) / 4 * 2) : ((n - 2 * LEAD) & 3) == 3 ? hslot((n - 2 * LEAD) / 4 * 2 + 1)
         : (n - 2 * LEAD) / 4 * 2 + ((n - 2 * LEAD) & 3) - 1;
}
template <int N> struct HS { static constexpr int v = hslot(N); };
struct Params {
    const u16* x; const u16* k; const u16* v; const u16* sk; const u16* sK; const u16* sD;
    const u16* w1; const u16* w2; const u16* gw; u16* out; char* ws;
};

__global__ void __launch_bounds__(512, 1) fused_kernel(Params p) {
    extern __shared__ __attribute__((aligned(16))) char smem[];
    const unsigned lds0 = (unsigned)(uintptr_t)(LAS char*)smem;
    const int tid = threadIdx.x, lane = tid & 63;
    const int w8 = __builtin_amdgcn_readfirstlane(tid >> 6), w = w8 & 3;
    const int b = blockIdx.x, xcd = b & 7, j = b >> 3;
    const int R = xcd * 4 + (j >> 3), C = j & 7;
    const bool gemm1 = j < 28;
    const int mt = (xcd >> 1) * 4 + j / 7, nt = (xcd & 1) * 7 + j % 7;
    const int cp = b;
    char* ws = p.ws;
    const rsrc_t wsr = __builtin_amdgcn_make_buffer_rsrc(ws, 0, WS_END, 0x00020000);
    const unsigned cnt = lds0 + L_CNT;
    const int h32 = lane >> 5, blk = (lane >> 4) & 1, q4 = (lane & 15) >> 2, pp = lane & 3;
    if (tid < 32) lds_w32(cnt + 4 * tid, 0u);
    LBAR();
    const rsrc_t w2r = __builtin_amdgcn_make_buffer_rsrc((void*)p.w2, 0, F_ * D_ * 2, 0x00020000);
    const unsigned w2off = (unsigned)(lane >> 3) * 1024u + (unsigned)(((lane & 7) ^ (((lane >> 4) & 1) << 2)) * 16);
    const unsigned w2s = (unsigned)(448 * w) * 1024u + C * 128u;
    const unsigned lsw = lds0 + w * 8192u;
    const unsigned myprog = cnt + C_PROG + 4 * w;

    if (w8 < 4) {
        const unsigned rho = 0;
        unsigned w2q[4];
#pragma unroll
        for (int q = 0; q < 4; ++q) w2q[q] = w2s + ((q + rho) & 3u) * (14u * 8192u);
#define W2P(i) (dma<SLOT(4 + ((i) >> 3)) + ((i) & 7) * 1024u, ((i) % 14) * 8192u>(w2off, w2r, w2q[(i) / 14], lsw))
#define W2G(g) do { W2P(8 * (g)); W2P(8 * (g) + 1); W2P(8 * (g) + 2); W2P(8 * (g) + 3); W2P(8 * (g) + 4); W2P(8 * (g) + 5); W2P(8 * (g) + 6); W2P(8 * (g) + 7); } while (0)
        if (gemm1) {
            const rsrc_t xr = __builtin_amdgcn_make_buffer_rsrc((void*)p.x, 0, S_ * D_ * 2, 0x00020000);
            const rsrc_t w1r = __builtin_amdgcn_make_buffer_rsrc((void*)p.w1, 0, F_ * D_ * 2, 0x00020000);
            const unsigned xoffE = (unsigned)(lane >> 3) * 1024u + (unsigned)(((lane & 7) ^ ((lane >> 4) & 7)) * 16);
            const unsigned xoffO = xoffE ^ 64u;
            const unsigned woff = (unsigned)(lane >> 4) * 3584u + (unsigned)((((lane & 15) ^ ((lane >> 4) << 2))) * 16);
            const unsigned woffs[4] = {woff, woff ^ 16u, woff ^ 32u, woff ^ 48u};
            const unsigned xs = (unsigned)(128 * mt + 32 * w) * 1024u, w1s = (unsigned)(16 * w) * 3584u + nt * 256u;
            const unsigned lw = lds0 + w * 4096u;
            const int rot = xcd;
            unsigned xss[8], wss[8];
#pragma unroll
            for (int s = 0; s < 8; ++s) { const unsigned sr_ = (unsigned)(s + rot) & 7u; xss[s] = xs + sr_ * 128u; wss[s] = w1s + sr_ * (64u * 3584u); }
#define G1P(s, i) do { if (((i) & 1) == 0) (dma<SLOT(s) + ((i) >> 1) * 1024u, ((i) >> 1) * 8192u>(((i) & 2) ? xoffO : xoffE, xr, xss[s], lw)); \
            else (dma<SLOT(s) + 16384u + ((i) >> 1) * 1024u, ((i) >> 1) * 4u * 3584u>(woffs[(i) >> 1], w1r, wss[s], lw)); } while (0)
#define G1S(s) do { G1P(s, 0); G1P(s, 1); G1P(s, 2); G1P(s, 3); G1P(s, 4); G1P(s, 5); G1P(s, 6); G1P(s, 7); } while (0)
            G1S(0); G1S(1);
            const int wn = w >> 1, wm = w & 1;
            unsigned aaddr[2][2], baddr[4];
#pragma unroll
            for (int a = 0; a < 2; ++a)
#pragma unroll
                for (int t = 0; t < 2; ++t) {
                    const int row = 8 * h32 + 4 * t + q4, ch = 4 * (2 * wn + a) + 2 * blk + (pp >> 1);
                    aaddr[a][t] = lds0 + 16384 + 256 * row + 16 * (ch ^ ((q4 << 2) | (2 * h32 + t))) + 8 * (pp & 1);
                }
#pragma unroll
            for (int ks = 0; ks < 4; ++ks) {
                const int row = 64 * wm + (lane & 31);
                baddr[ks] = lds0 + row * 128 + 16 * ((2 * ks + h32) ^ ((row >> 1) & 7));
            }
            f32x16 acc[2][2];
#pragma unroll
            for (int a = 0; a < 2; ++a)
#pragma unroll
                for (int c = 0; c < 2; ++c) acc[a][c] = f32x16{};
            bf16x8 af[3][2], bfr[3][2];
            unsigned aah[2][2], bah[4];
#pragma unroll
            for (int i = 0; i < 4; ++i) { aah[i >> 1][i & 1] = aaddr[i >> 1][i & 1] + 65536u; bah[i] = baddr[i] + 65536u; asm volatile("" : "+v"(aah[i >> 1][i & 1]), "+v"(bah[i])); }
#define G1_AA(a, t, so, ks) ((so) >= 65536u ? aah[a][t] + ((so) - 65536u) + (ks) * 4096 : aaddr[a][t] + (so) + (ks) * 4096)
#define G1_BA(c, so, ks) ((so) >= 65536u ? bah[ks] + ((so) - 65536u) + (c) * 4096 : baddr[ks] + (so) + (c) * 4096)
#define G1_RD(buf, i, so, ks) do { af[buf][i] = cat8(vtr(G1_AA(i, 0, so, ks)), vtr(G1_AA(i, 1, so, ks))); bfr[buf][i] = lds_r128(G1_BA(i, so, ks)); SBAR(); } while (0)
#define G1_M(buf, a, c) do { acc[a][c] = MFMA32(af[buf][a], bfr[buf][c], acc[a][c]); SBAR(); } while (0)
#define G1_GRP(buf, F0, F1, F2, F3) do { G1_M(buf, 0, 0); F0; SBAR(); G1_M(buf, 0, 1); F1; SBAR(); G1_M(buf, 1, 0); F2; SBAR(); G1_M(buf, 1, 1); F3; SBAR(); } while (0)
            WAITV(8);
            cnt_set(myprog, 1u);
            prog_wait(cnt + C_PROG, 1u);
            G1_RD(0, 0, SLOT(0), 0); G1_RD(0, 1, SLOT(0), 0); G1_RD(1, 0, SLOT(0), 1); G1_RD(1, 1, SLOT(0), 1);
            u32x4 cv;
#define NOP ((void)0)
#define BUF(s, ks) ((4 * (s) + (ks)) % 3)
#define G1_STAGE(s, D0, D1, D2, D3, D4, D5, D6, D7, WV) do {                                                                 \
            G1_GRP(BUF(s, 0), G1_RD(BUF(s, 2), 0, SLOT(s), 2), D0, G1_RD(BUF(s, 2), 1, SLOT(s), 2), D1);                      \
            G1_GRP(BUF(s, 1), G1_RD(BUF(s, 3), 0, SLOT(s), 3), D2, G1_RD(BUF(s, 3), 1, SLOT(s), 3), D3);                      \
            WV; cnt_set(myprog, (s) + 2u); SBAR();                                                                            \
            G1_GRP(BUF(s, 2), D4, cv = lds_r128u(cnt + C_PROG), D5, NOP);                                                     \
            if (min4(cv) < (s) + 2u) prog_wait(cnt + C_PROG, (s) + 2u);                                                       \
            if ((s) < 7) { G1_RD(BUF(s, 4), 0, SLOT((s) + 1), 0);                                                             \
                G1_GRP(BUF(s, 3), G1_RD(BUF(s, 4), 1, SLOT((s) + 1), 0); D6, G1_RD(BUF(s, 5), 0, SLOT((s) + 1), 1), G1_RD(BUF(s, 5), 1, SLOT((s) + 1), 1), D7); } \
            else G1_GRP(BUF(s, 3), D6, NOP, D7, NOP); } while (0)
#define G1_STD(s) G1_STAGE(s, G1P(s + 3, 0), G1P(s + 3, 1), G1P(s + 3, 2), G1P(s + 3, 3), G1P(s + 3, 4), G1P(s + 3, 5), G1P(s + 3, 6), G1P(s + 3, 7), WAITV(12))
            G1_STAGE(0, G1P(2, 0); G1P(2, 1), G1P(2, 2), G1P(2, 3); G1P(2, 4), G1P(2, 5), G1P(2, 6); G1P(2, 7), G1P(3, 0), G1P(3, 1); G1P(3, 2), G1P(3, 3), WAITV(6));
            G1_STAGE(1, G1P(3, 4); G1P(3, 5), G1P(3, 6), G1P(3, 7); G1P(4, 0), G1P(4, 1), G1P(4, 2); G1P(4, 3), G1P(4, 4), G1P(4, 5); G1P(4, 6), G1P(4, 7), WAITV(10));
            G1_STD(2); G1_STD(3);
            G1_STD(4);
            G1_STAGE(5, W2P(0), W2P(1), W2P(2), W2P(3), W2P(4), W2P(5), W2P(6), W2P(7), WAITV(12));
            G1_STAGE(6, W2P(8), W2P(9), W2P(10), W2P(11), W2P(12), W2P(13), W2P(14), W2P(15), WAITV(12));
            G1_STAGE(7, W2P(16), W2P(17), W2P(18), W2P(19), W2P(20), W2P(21), W2P(22), W2P(23), NOP);
#pragma unroll
            for (int a = 0; a < 2; ++a)
#pragma unroll
                for (int c = 0; c < 2; ++c)
#pragma unroll
                    for (int s = 0; s < 2; ++s) {
                        u32x4 o;
#pragma unroll
                        for (int i = 0; i < 4; ++i)
                            o[i] = __builtin_bit_cast(unsigned, __builtin_elementwise_max(__builtin_bit_cast(s16x2, cvtpk(acc[a][c][8 * s + 2 * i], acc[a][c][8 * s + 2 * i + 1])), s16x2{0, 0}));
                        const unsigned mb = 4 * mt + 2 * wm + c, ksg = 8 * nt + 4 * wn + 2 * a + s;
                        __builtin_amdgcn_raw_buffer_store_b128(o, wsr, lane * 16u, WS_H + (mb * 112u + ksg) * 1024u, 16);
                        asm volatile("s_nop 1" :: "v"(o) : "memory");
                        SBAR();
                    }
        } else {
            const int sp = xcd * 4 + (j - 28);
            {
                const unsigned pfs = (unsigned)(sp * 4 + w) * 14336u, pfo = lane * 16u;
#define PF(i) (dma<SLOT(7) + ((i) & 7) * 1024u, (i) * 1024u>(pfo, w2r, pfs, lsw))
                PF(0); PF(1); PF(2); PF(3); PF(4); PF(5); PF(6); PF(7); PF(8); PF(9); PF(10); PF(11); PF(12); PF(13);
            }
            const u32x4 gwv = *(const u32x4*)(p.gw + lane * 8);
            u32x4 xv[16];
#pragma unroll
            for (int i = 0; i < 16; ++i) xv[i] = *(const u32x4*)(p.x + (64 * sp + 16 * w + i) * D_ + lane * 8);
            CFENCE();
            float r[16];
#pragma unroll
            for (int i = 0; i < 16; ++i) r[i] = dot2(xv[i][3], gwv[3], dot2(xv[i][2], gwv[2], dot2(xv[i][1], gwv[1], dot2(xv[i][0], gwv[0], 0.f))));
#pragma unroll
            for (int i = 0; i < 8; ++i) { auto s = __builtin_amdgcn_permlane32_swap(__float_as_uint(r[i]), __float_as_uint(r[i + 8]), false, false); r[i] = __uint_as_float(s[0]) + __uint_as_float(s[1]); }
#pragma unroll
            for (int i = 0; i < 4; ++i) { auto s = __builtin_amdgcn_permlane16_swap(__float_as_uint(r[i]), __float_as_uint(r[i + 4]), false, false); r[i] = __uint_as_float(s[0]) + __uint_as_float(s[1]); }
#pragma unroll
            for (int o = 8; o >= 1; o >>= 1)
#pragma unroll
                for (int i = 0; i < 4; ++i) r[i] += __shfl_xor(r[i], o);
            const float gs = pp == 0 ? r[0] : pp == 1 ? r[1] : pp == 2 ? r[2] : r[3];
            const float g = 1.f / (1.f + __expf(-gs));
            __builtin_amdgcn_raw_buffer_store_b32(__float_as_uint(g), wsr, q4 == 0 ? WS_G + (unsigned)(64 * sp + 16 * w + 4 * (lane >> 4) + pp) * 4u : 0xfffffff0u, 0, 16);
            W2G(0); W2G(1); W2G(2);
            WAITV(0);
        }
        const unsigned w2sw = 128 * (4 * h32 + q4) + 16 * ((2 * blk + (pp >> 1)) ^ ((q4 >> 1) << 2)) + 8 * (pp & 1);
        unsigned wa[2][2];
#pragma unroll
        for (int a = 0; a < 2; ++a) {
            const unsigned o = w2sw ^ (unsigned)((a ^ (w >> 1)) * 64);
            wa[a][0] = lsw + o; wa[a][1] = lsw + 65536u + o;
        }
#define W2ADDR(a, kk) (wa[a][SLOT(4 + ((kk) >> 2)) >> 16] + (SLOT(4 + ((kk) >> 2)) & 65535u) + ((kk) & 3) * 2048u)
#define W2FRAG(a, kk) cat8(vtr(W2ADDR(a, kk)), vtr(W2ADDR(a, kk) + 1024u))
        bf16x8 rg[24 + 2 * LEAD];
#define W2GET(g) do { _Pragma("unroll") for (int kk = 4 * (g); kk < 4 * (g) + 4; ++kk) _Pragma("unroll") for (int a = 0; a < 2; ++a) rg[2 * kk + a] = W2FRAG(a, kk); } while (0)
        W2G(3);
        WAITV(16);
        W2GET(0); W2GET(1); W2GET(2);
        cnt_wait(cnt + C_KV, 4u);
        const u64 tag = lds_r64(cnt + C_TAG);
        WAITV(8);
        if (cnt_add_rtn(cnt + C_H) == 3u && lane == 0)
            __hip_atomic_store((gu64*)(ws + (gemm1 ? WS_FH + (unsigned)(mt * 16 + nt) * 8u : WS_FG + (unsigned)(xcd * 4 + j - 28) * 8u)), tag, RLX_AGENT);
        if (b == 255 && w == 0) {
            const gu64* fp = (const gu64*)(ws + WS_FKV + (unsigned)lane * 8u);
#pragma unroll 1
            for (unsigned spins = 0; spins < SPIN_CAP; ++spins) {
                const u64 f0 = __hip_atomic_load(fp, RLX_AGENT), f1 = __hip_atomic_load(fp + 64, RLX_AGENT), f2 = __hip_atomic_load(fp + 128, RLX_AGENT), f3 = __hip_atomic_load(fp + 192, RLX_AGENT);
                if (__all(f0 == tag && f1 == tag && f2 == tag && f3 == tag)) break;
                __builtin_amdgcn_s_sleep(1);
            }
            if (lane == 0) __hip_atomic_store((gu64*)(ws + WS_EPOCH), tag, RLX_AGENT);
        }
        const gu64* fh = (const gu64*)(ws + (lane >= 32 ? WS_FY + (unsigned)(32 * C + lane - 32) * 8u : lane == 31 ? WS_FG + (unsigned)R * 8u
                                                                                                     : WS_FH + (unsigned)((R >> 1) * 16 + min(lane, 13)) * 8u));
        W2G(4); W2G(5);
        W2G(6);
        if (w == 0) {
            u64 m = 0;
            mask_wait(cnt + C_Y, 4u);
            __builtin_amdgcn_s_sleep(PSH0);
            if (!gemm1) __builtin_amdgcn_s_sleep(PSS);
#pragma unroll 1
            for (unsigned spins = 0; spins < SPIN_CAP && (m & 0x3fffu) != 0x3fffu; ++spins) {
                if (spins) { if (__popc((unsigned)m & 0x3fffu) < 8) __builtin_amdgcn_s_sleep(PSF); else __builtin_amdgcn_s_sleep(PSH); }
                m = __ballot(__hip_atomic_load(fh, RLX_AGENT) == tag);
                cnt_set(cnt + C_HM, (unsigned)m);
            }
            cnt_set(cnt + C_YM, (m >> 31) == 0x1ffffffffull ? 1u : 2u);
            CFENCE();
        } else mask_wait(cnt + C_HM, 15u << ((448 * w) >> 7));
        f32x16 acc[2][2];
#pragma unroll
        for (int a = 0; a < 2; ++a)
#pragma unroll
            for (int c = 0; c < 2; ++c) acc[a][c] = f32x16{};
        {
                        const unsigned hs0 = WS_H + ((2 * R + (w & 1)) * 112u + 28 * w) * 1024u, hs1 = WS_H + ((2 * R + ((w & 1) ^ 1)) * 112u + 28 * w) * 1024u;
            unsigned hsq[2][4];
#pragma unroll
            for (int q = 0; q < 4; ++q) { hsq[0][q] = hs0 + ((q + rho) & 3u) * 7168u; hsq[1][q] = hs1 + ((q + rho) & 3u) * 7168u; }
            bf16x8 wf[2][2];
            const unsigned hvo = lane * 16u;
#define HLD0(n) do { unsigned t_; asm volatile("s_add_u32 %1, %4, %5\n\tbuffer_load_dwordx4 %0, %2, %3, %1 offen sc1" : "=v"(rg[HS<(n)>::v]), "=&s"(t_) \
                : "v"(hvo), "s"(wsr), "s"(hsq[(n) & 1][((n) / 14) & 3]), "n"((((n) % 14) >> 1) * 1024) : "memory", "scc"); } while (0)
#define HLD(n) do { if ((n) < 56) { unsigned t_; asm volatile("s_add_u32 %1, %4, %5\n\tbuffer_load_dwordx4 %0, %2, %3, %1 offen sc1" : "+v"(rg[HS<(n)>::v]), "=&s"(t_) \
                : "v"(hvo), "s"(wsr), "s"(hsq[(n) & 1][((n) / 14) & 3]), "n"((((n) % 14) >> 1) * 1024) : "memory", "scc"); SBAR(); } } while (0)
#define HWAIT(n, issued) asm volatile("s_waitcnt vmcnt(%1)" : "+v"(rg[HS<(n)>::v]) : "n"(((issued) < 56 ? (issued) : 56) - (n) - 1) : "memory")
            HLD0(0); HLD0(1); HLD0(2); HLD0(3); HLD0(4); HLD0(5); HLD0(6); HLD0(7); HLD0(8); HLD0(9); HLD0(10); HLD0(11); HLD0(12); HLD0(13); HLD0(14); HLD0(15);
            SBAR();
#define G2(ks) do {                                                                                                             \
                if ((ks) >= 11 && (ks) < 27) { wf[((ks) + 1) & 1][0] = W2FRAG(0, (ks) + 1); wf[((ks) + 1) & 1][1] = W2FRAG(1, (ks) + 1); SBAR(); } \
                const bf16x8 a0 = (ks) < 12 ? rg[(ks) < 12 ? 2 * (ks) : 0] : wf[(ks) & 1][0], a1 = (ks) < 12 ? rg[(ks) < 12 ? 2 * (ks) + 1 : 0] : wf[(ks) & 1][1]; \
                HWAIT(2 * (ks), 2 * LEAD + 4 * (ks));                                                                           \
                acc[0][0] = MFMA32(a0, rg[HS<2 * (ks)>::v], acc[0][0]); SBAR();                                                 \
                acc[1][0] = MFMA32(a1, rg[HS<2 * (ks)>::v], acc[1][0]); SBAR();                                                 \
                HLD(2 * LEAD + 4 * (ks));                                                                                       \
                HWAIT(2 * (ks) + 1, 2 * LEAD + 4 * (ks) + 1);                                                                   \
                acc[0][1] = MFMA32(a0, rg[HS<2 * (ks) + 1>::v], acc[0][1]); SBAR();                                             \
                HLD(2 * LEAD + 4 * (ks) + 1);                                                                                   \
                acc[1][1] = MFMA32(a1, rg[HS<2 * (ks) + 1>::v], acc[1][1]); SBAR();                                             \
                HLD(2 * LEAD + 4 * (ks) + 2); HLD(2 * LEAD + 4 * (ks) + 3); } while (0)
            G2(0); G2(1); G2(2); G2(3); G2(4); G2(5); G2(6); G2(7); G2(8); G2(9); G2(10); G2(11); G2(12); G2(13);
            G2(14); G2(15); G2(16); G2(17); G2(18); G2(19); G2(20); G2(21); G2(22); G2(23); G2(24); G2(25); G2(26); G2(27);
        }
#pragma unroll
        for (int o = 1; o < 4; ++o)
#pragma unroll
            for (int g = 0; g < 4; ++g) {
                const f32x16 v = acc[o >> 1][o & 1];
                lds_w128f(lsw + (o == 3 ? 32768u : (o - 1) * 4096u) + (unsigned)(g * 64 + lane) * 16u, f32x4{v[4 * g], v[4 * g + 1], v[4 * g + 2], v[4 * g + 3]});
            }
        LBAR();
        {
            const unsigned kc = lds0 + L_KC + (w >> 1) * (2 * KCS);
            const float og = __uint_as_float(lds_r32(lds0 + L_U + w * 2304u + 2048u + lane * 4u));
            u32x2 o2[4];
#pragma unroll
            for (int g = 0; g < 4; ++g) {
                f32x4 s = f32x4{acc[0][0][4 * g], acc[0][0][4 * g + 1], acc[0][0][4 * g + 2], acc[0][0][4 * g + 3]};
#pragma unroll
                for (int o = 1; o < 4; ++o) s += lds_r128f(lds0 + (unsigned)((w ^ o) * 8192) + (o == 3 ? 32768u : (o - 1) * 4096u) + (unsigned)(g * 64 + lane) * 16u);
                const f32x4 ga = lds_r128f(kc + (w & 1) * 4096u + (unsigned)(g * 64 + lane) * 16u);
                o2[g][0] = cvtpk(fmaf(og, s[0], ga[0]), fmaf(og, s[1], ga[1]));
                o2[g][1] = cvtpk(fmaf(og, s[2], ga[2]), fmaf(og, s[3], ga[3]));
            }
            u16* op = p.out + (64 * R + 32 * (w & 1) + (lane & 31)) * D_ + 64 * C + 32 * (w >> 1) + 8 * h32;
#pragma unroll
            for (int g = 0; g < 4; g += 2) {
                auto s0 = __builtin_amdgcn_permlane32_swap(o2[g][0], o2[g + 1][0], false, false);
                auto s1 = __builtin_amdgcn_permlane32_swap(o2[g][1], o2[g + 1][1], false, false);
                *(u32x4*)(op + 8 * g) = u32x4{s0[0], s1[0], s0[1], s1[1]};
            }
        }
    } else {
        const int tb = tid - 256;
        __builtin_amdgcn_s_setprio(3);
        u64 tag = 0;
#define EPLD() do { if (w == 1) asm volatile("global_load_dwordx2 %0, %1, off sc1" : "+v"(tag) : "v"((const gu64*)(ws + WS_EPOCH)) : "memory"); } while (0)
        const rsrc_t kr_ = __builtin_amdgcn_make_buffer_rsrc((void*)p.k, 0, S_ * D_ * 2, 0x00020000);
        u32x4 kr[5], vr[2], sr[4], kA[2]; unsigned kB[2];
        const int tp = tb >> 3, cc = tb & 7, t0 = 64 * R + 2 * tp, ch = 64 * C + 8 * cc;
#pragma unroll
        for (int i = 0; i < 5; ++i) { const int t = t0 - 3 + i; kr[i] = bld128(kr_, t < 0 ? 0x80000000u : (unsigned)(t * D_ + ch) * 2u, 0); }
#pragma unroll
        for (int i = 0; i < 2; ++i) vr[i] = *(const u32x4*)(p.v + (t0 + i) * D_ + ch);
#pragma unroll
        for (int i = 0; i < 4; ++i) sr[i] = *(const u32x4*)(p.sk + ch * 4 + i * 8);
        CFENCE();
#pragma unroll
        for (int h = 0; h < 2; ++h) {
            const u16* kp = p.sK + (2 * cp + h) * S_;
            kA[h] = *(const u32x4*)(kp + 2040 - 8 * tb);
            kB[h] = *(const unsigned*)(kp + (tb == 255 ? 0 : 2038 - 8 * tb));
        }
        CFENCE();
#pragma unroll
        for (int e = 0; e < 8; ++e) {
            const unsigned s01 = sr[e >> 1][(e & 1) * 2], s23 = sr[e >> 1][(e & 1) * 2 + 1];
            const unsigned sel = (e & 1) ? 0x03020706u : 0x01000504u;
            unsigned pr[4];
#pragma unroll
            for (int i = 0; i < 4; ++i) pr[i] = __builtin_amdgcn_perm(kr[i + 1][e >> 1], kr[i][e >> 1], sel);
            const float r0 = dot2(pr[2], s01, dot2(pr[0], s23, 0.f)), r1 = dot2(pr[3], s01, dot2(pr[1], s23, 0.f));
            const float va = (e & 1) ? hi_f(vr[0][e >> 1]) : lo_f(vr[0][e >> 1]);
            const float vb = (e & 1) ? hi_f(vr[1][e >> 1]) : lo_f(vr[1][e >> 1]);
            lds_w32(lds0 + L_U + (unsigned)((8 * cc + e) * 32 + (tp ^ (cc << 2))) * 4, cvtpk(r0 * va, r1 * vb));
        }
        cnt_add(cnt + C_TR);
        cnt_wait(cnt + C_TR, 4u);
#pragma unroll
        for (int i = 0; i < 2; ++i) {
            const int row = (tb >> 3) + 32 * i, q = tb & 7;
            const u32x4 vv = lds_r128u(lds0 + L_U + (unsigned)(row * 128 + ((q ^ (row >> 3)) * 16)));
            __builtin_amdgcn_raw_buffer_store_b128(vv, wsr, WS_KVT + (unsigned)((64 * C + row) * S_ + 64 * R + 8 * q) * 2, 0, 16);
        }
        CFENCE();
#pragma unroll
        for (int h = 0; h < 2; ++h) {
            const unsigned W[5] = {tb == 255 ? 0u : kB[h], kA[h][0], kA[h][1], kA[h][2], kA[h][3]};
            unsigned Rr[5], Mm[5];
#pragma unroll
            for (int i = 1; i < 5; ++i) { Rr[i] = __builtin_amdgcn_alignbit(W[i], W[i], 16); Mm[i] = __builtin_amdgcn_perm(W[i - 1], W[i], 0x07060100u); }
            const unsigned kb = lds0 + L_KC + h * 2 * KCS + tb * 16;
            lds_w128(kb, u32x4{Rr[4], Rr[3], Rr[2], Rr[1]});
            lds_w128(kb + KCS, u32x4{Mm[4], Mm[3], Mm[2], Mm[1]});
        }
        if (tb < 16) lds_w128(lds0 + L_KC + (tb >> 2) * KCS + 4096 + (tb & 3) * 16, u32x4{0u, 0u, 0u, 0u});
        CFENCE();
        EPLD();
        asm volatile("s_waitcnt vmcnt(0)" : "+v"(tag) :: "memory");
        tag += 0x9E3779B97F4A7C15ull;
        if (tb == 64) *(LAS u64*)(uintptr_t)(cnt + C_TAG) = tag;
        if (cnt_add_rtn(cnt + C_KV) == 3u && lane == 0) __hip_atomic_store((gu64*)(ws + WS_FKV + (unsigned)(C * 32 + R) * 8u), lds_r64(cnt + C_TAG), RLX_AGENT);
        const int tf = 64 * R + 32 * (w & 1) + (lane & 31), chf = 64 * C + 32 * (w >> 1) + 4 * h32;
        u32x2 qv[4];
#pragma unroll
        for (int g = 0; g < 4; ++g) qv[g] = *(const u32x2*)(p.x + tf * D_ + chf + 8 * g);
        const int cw = w >> 1, rh = w & 1, a16 = lane & 15, kq = lane >> 4;
        const float dch = lo_f(p.sD[2 * cp + cw]);
        const gu64* fk = (const gu64*)(ws + WS_FKV + (unsigned)((cp >> 5) * 32 + (lane & 31)) * 8u);
        if (w == 1) { __builtin_amdgcn_s_sleep(PSK); flag_wait(fk, tag); cnt_set(cnt + C_KM, 1u); } else mask_wait(cnt + C_KM, 1u);
        u32x4 ur[2];
#pragma unroll
        for (int h = 0; h < 2; ++h) ur[h] = bld128(wsr, WS_KVT + (unsigned)((2 * cp + h) * S_ + 512 * w + 8 * lane) * 2, 1);
        cnt_wait(cnt + C_KV, 4u);
        if (tb < 64) lds_w128(lds0 + L_U + (tb >> 5) * 4608 + (tb & 31) * 16, u32x4{0u, 0u, 0u, 0u});
#pragma unroll
        for (int h = 0; h < 2; ++h) lds_w128(lds0 + L_U + h * 4608 + 512 + (512 * w + 8 * lane) * 2, ur[h]);
        cnt_add(cnt + C_U);
        cnt_wait(cnt + C_U, 4u);
        __builtin_amdgcn_s_setprio(0);
        f32x4 cacc[8];
        {
            const unsigned kaddr = lds0 + L_KC + cw * 2 * KCS + ((~a16) & 1) * KCS + 2 * (2052 + ((~a16) & 2) - 4 * (a16 >> 2) - 8 * kq) - 256 * rh - 64 * 63;
            unsigned kab[4];
#pragma unroll
            for (int i = 0; i < 4; ++i) { kab[i] = kaddr + 1024 * i; asm volatile("" : "+v"(kab[i])); }
            const unsigned uaddr = lds0 + L_U + cw * 4608 + 2 * (256 + 16 * a16 + 8 - 8 * kq) - 448 - 256 * rh;
#pragma unroll
            for (int qq = 0; qq < 8; ++qq) cacc[qq] = f32x4{0.f, 0.f, 0.f, 0.f};
            bf16x8 A[2][8], B[2][8];
#define CV_RD(buf, rr, e) do { const unsigned ko = 64 * (63 - 8 * (e)) - 64 * (rr), ka = kab[ko >> 10] + (ko & 1023u);                        \
            A[buf][e] = __builtin_bit_cast(bf16x8, u32x4{lds_r32(ka), lds_r32(ka + 4), lds_r32(ka + 8), lds_r32(ka + 12)});                          \
            B[buf][e] = lds_r128(uaddr + 512 * (e) + 64 * (7 - (rr))); } while (0)
#pragma unroll
            for (int e = 0; e < 8; ++e) CV_RD(0, 0, e);
            SBAR();
#pragma unroll
            for (int rr = 0; rr < 4; ++rr) {
#pragma unroll
                for (int m = 0; m < 8; ++m) {
                    if (rr < 3) CV_RD((rr + 1) & 1, rr + 1, m);
#pragma unroll
                    for (int e = 0; e <= m; ++e) if (e + m < 8) cacc[e + m] = MFMA16(A[rr & 1][e], B[rr & 1][m], cacc[e + m]);
#pragma unroll
                    for (int e = 0; e < m; ++e) if (e + m < 8) cacc[e + m] = MFMA16(A[rr & 1][m], B[rr & 1][e], cacc[e + m]);
                    SBAR();
                }
            }
        }
        __builtin_amdgcn_s_setprio(3);
        f32x4 give[4], keep[4];
#pragma unroll
        for (int i = 0; i < 4; ++i)
#pragma unroll
            for (int e = 0; e < 4; ++e) { const float lo = cacc[i][e], hi = cacc[4 + i][e]; give[i][e] = rh ? lo : hi; keep[i][e] = rh ? hi : lo; }
        const unsigned kc = lds0 + L_KC + cw * (2 * KCS);
        cnt_add(cnt + C_PA + 4 * cw);
        cnt_wait(cnt + C_PA + 4 * cw, 2u);
        {
            const unsigned eo = kc + (rh ^ 1) * 4096u + lane * 16u;
#pragma unroll
            for (int i = 0; i < 4; ++i) lds_w128f(eo + i * 1024u, give[i]);
        }
        cnt_add(cnt + C_PB + 4 * cw);
        u64 uu[4];
        const int ty = 1024 * rh + 16 * a16 + 4 * kq;
#pragma unroll
        for (int i = 0; i < 4; ++i) uu[i] = lds_r64(lds0 + L_U + cw * 4608 + 512 + 2 * (ty + 256 * i));
        cnt_wait(cnt + C_PB + 4 * cw, 2u);
        u32x2 yo[4];
#pragma unroll
        for (int i = 0; i < 4; ++i) {
            const f32x4 y = lds_r128f(kc + rh * 4096u + lane * 16u + i * 1024u) + keep[i];
            const unsigned u0 = (unsigned)uu[i], u1 = (unsigned)(uu[i] >> 32);
            yo[i] = u32x2{cvtpk(fmaf(lo_f(u0), dch, y[0]), fmaf(hi_f(u0), dch, y[1])), cvtpk(fmaf(lo_f(u1), dch, y[2]), fmaf(hi_f(u1), dch, y[3]))};
        }
#pragma unroll
        for (int i = 0; i < 4; i += 2) {
            auto s0 = __builtin_amdgcn_permlane16_swap(yo[i][0], yo[i + 1][0], false, false);
            auto s1 = __builtin_amdgcn_permlane16_swap(yo[i][1], yo[i + 1][1], false, false);
            __builtin_amdgcn_raw_buffer_store_b128(u32x4{s0[0], s1[0], s0[1], s1[1]}, wsr,
                WS_Y + (unsigned)((2 * cp + cw) * S_ + 1024 * rh + 256 * (i + (kq & 1)) + 16 * a16 + 8 * (kq >> 1)) * 2u, 0, 16);
        }
        CFENCE();
        WAITV(0);
        if (cnt_add_rtn(cnt + C_Y) == 3u && lane == 0) __hip_atomic_store((gu64*)(ws + WS_FY + (unsigned)cp * 8u), lds_r64(cnt + C_TAG), RLX_AGENT);
        const gu64* fy = (const gu64*)(ws + (lane < 32 ? WS_FY + (unsigned)(32 * C + lane) * 8u : WS_FG + (unsigned)R * 8u));
        if (w == 1) {
            unsigned ym = 0;
#pragma unroll 1
            for (unsigned spins = 0; spins < SPIN_CAP * 8 && ym == 0; ++spins) { CFENCE(); ym = __builtin_amdgcn_readfirstlane(lds_r32(cnt + C_YM)); __builtin_amdgcn_s_sleep(1); }
            CFENCE();
            if (ym != 1u) { __builtin_amdgcn_s_sleep(PSY); flag_wait(fy, tag); cnt_set(cnt + C_YM, 1u); }
        } else mask_wait(cnt + C_YM, 1u);
        u32x4 yr[2];
#pragma unroll
        for (int i = 0; i < 2; ++i) yr[i] = bld128(wsr, WS_Y + (unsigned)((64 * C + 32 * cw + 16 * i + (lane >> 2)) * S_ + 64 * R + 32 * rh + 8 * pp) * 2u, 1);
        const float gt = __uint_as_float(__builtin_amdgcn_raw_buffer_load_b32(wsr, WS_G + (unsigned)tf * 4u, 0, 16));
        const unsigned stg = lds0 + L_U + w * 2304u;
        cnt_wait(cnt + C_Y, 4u);
#pragma unroll
        for (int i = 0; i < 2; ++i) lds_w128(stg + i * 1024u + lane * 16u, yr[i]);
        CFENCE();
#pragma unroll
        for (int g = 0; g < 4; ++g) {
            const u32x2 y = __builtin_bit_cast(u32x2, vtr(stg + (unsigned)(8 * g + 4 * h32 + q4) * 64u + blk * 32u + pp * 8u));
            lds_w128f(kc + rh * 4096u + (unsigned)(g * 64 + lane) * 16u,
                      f32x4{gt * lo_f(qv[g][0]) * lo_f(y[0]), gt * hi_f(qv[g][0]) * hi_f(y[0]), gt * lo_f(qv[g][1]) * lo_f(y[1]), gt * hi_f(qv[g][1]) * hi_f(y[1])});
        }
        lds_w32(stg + 2048u + lane * 4u, __float_as_uint(1.f - gt));
        LBAR();
    }
}

extern "C" void kernel_launch(void* const* d_in, const int* in_sizes, int n_in, void* d_out, int out_size, void* d_ws, size_t ws_size,
                              hipStream_t stream) {
    Params p;
    p.x = (const u16*)d_in[0]; p.k = (const u16*)d_in[1]; p.v = (const u16*)d_in[2]; p.sk = (const u16*)d_in[3];
    p.sK = (const u16*)d_in[4]; p.sD = (const u16*)d_in[5]; p.w1 = (const u16*)d_in[7]; p.w2 = (const u16*)d_in[8];
    p.gw = (const u16*)d_in[9]; p.out = (u16*)d_out; p.ws = (char*)d_ws;
    (void)hipFuncSetAttribute((const void*)fused_kernel, hipFuncAttributeMaxDynamicSharedMemorySize, L_END);
    fused_kernel<<<256, 512, L_END, stream>>>(p);
}
```
